```python
import torch

def _softmax(x, axis=-1):
    return torch.softmax(x, dim=axis)
def _gelu(x):
    return torch.nn.functional.gelu(x, approximate="tanh")
def _silu(x):
    return torch.nn.functional.silu(x)
def _sigmoid(x):
    return torch.sigmoid(x)
def _layernorm(x, axis=-1, eps=1e-5):
    m = x.mean(dim=axis, keepdim=True)
    v = x.var(dim=axis, keepdim=True, unbiased=False)
    return (x - m) / torch.sqrt(v + eps)
def _scatter_add(tgt, idx, src, axis=0):
    return tgt.clone().index_add_(axis, idx, src)
def _segment_sum(src, seg, n):
    out = torch.zeros((n,) + src.shape[1:], dtype=src.dtype)
    return out.index_add_(0, seg, src)
def _pad(x, pw):
    flat = tuple(v for pair in reversed(pw) for v in pair)
    return torch.nn.functional.pad(x, flat)

kernel_name = 'concat_heads__ssm_conv_s4d__softmax_full_mha__prompt'

def setup_inputs():

    _w = (lambda a: (lambda t: t.to(torch.bfloat16) if t.is_floating_point() else t)(torch.as_tensor(a)))
    def _A_setup():
        Bsz, L, D, N = 1, 2048, 512, 128
        return {
            "u": _w((lambda *s: torch.randn(*s, dtype=torch.bfloat16) * 0.05)(Bsz, L, D)),
            "log_A_real": _w((lambda *s: torch.randn(*s, dtype=torch.bfloat16) * 6.4)(D, N) * 0.5),
            "A_imag": _w((lambda *s: torch.randn(*s, dtype=torch.bfloat16) * 0.1)(D, N) * 3.14),
            "C_real": _w((lambda *s: torch.randn(*s, dtype=torch.bfloat16) * 0.4)(D, N) / N ** 0.5),
            "C_imag": _w((lambda *s: torch.randn(*s, dtype=torch.bfloat16) * 0.4)(D, N) / N ** 0.5),
            "log_dt": _w((lambda *s: torch.randn(*s, dtype=torch.bfloat16) * 0.1)(D) * 0.5 - 4.0),
            "D_skip": _w((lambda *s: torch.randn(*s, dtype=torch.bfloat16) * 0.1)(D)),
        }
    _A_in = _A_setup()

    def _B_setup():
        B, S, D = 1, 2048, 512
        return {
            "x": _w((lambda *s: torch.randn(*s, dtype=torch.bfloat16) * 0.2)(B, S, D)),
            "wqkv": _w((lambda *s: torch.randn(*s, dtype=torch.bfloat16) * 0.2)(D, 3 * D)),
            "wo": _w((lambda *s: torch.randn(*s, dtype=torch.bfloat16) * 0.05)(D, D)),
        }
    _B_in = _B_setup()

    return {
        "A_u": _A_in["u"],
        "A_log_A_real": _A_in["log_A_real"],
        "A_A_imag": _A_in["A_imag"],
        "A_C_real": _A_in["C_real"],
        "A_C_imag": _A_in["C_imag"],
        "A_log_dt": _A_in["log_dt"],
        "A_D_skip": _A_in["D_skip"],
        "B_x": _B_in["x"],
        "B_wqkv": _B_in["wqkv"],
        "B_wo": _B_in["wo"],
    }

def reference(A_u, A_log_A_real, A_A_imag, A_C_real, A_C_imag, A_log_dt, A_D_skip, B_x, B_wqkv, B_wo):
    def _A_ref(u, log_A_real, A_imag, C_real, C_imag, log_dt, D_skip):
        L = u.shape[1]
        dt = torch.exp(log_dt)
        A = -torch.exp(log_A_real) + 1j * A_imag
        C = (C_real + 1j * C_imag) * dt[:, None]
        pos = (lambda x, dt: x.to(dt))(torch.arange(L), log_dt.dtype)
        P = torch.exp((A * dt[:, None])[:, :, None] * pos[None, None, :])
        K = 2.0 * torch.einsum("dn,dnl->dl", C, P).real
        L2 = 2 * L
        u_t = (lambda x, *d: x.permute(*d))(u, 0, 2, 1)
        Kf = (lambda x, *a, **k: torch.fft.rfft(x.float(), *a, **k))(K, L2)
        Uf = (lambda x, *a, **k: torch.fft.rfft(x.float(), *a, **k))(u_t, L2)
        y = (lambda x, *a, **k: torch.fft.irfft(x, *a, **k).to(torch.bfloat16))(Uf * Kf[None, :, :], L2)[:, :, :L]
        y = (lambda x, *d: x.permute(*d))(y, 0, 2, 1)
        return y + u * D_skip[None, None, :]

    def _B_ref(x, wqkv, wo):
        B, S, D = x.shape
        H = 32; Dh = D // H
        qkv = (lambda x, *d: x.permute(*d))((x @ wqkv).reshape(B, S, 3, H, Dh), 2, 0, 3, 1, 4)
        q, k, v = qkv[0], qkv[1], qkv[2]
        scale = Dh ** -0.5
        scores = torch.einsum("bhqd,bhkd->bhqk", q, k) * scale
        p = _softmax(scores, axis=-1)
        o = torch.einsum("bhqk,bhkd->bhqd", p, v)
        return (lambda x, *d: x.permute(*d))(o, 0, 2, 1, 3).reshape(B, S, D) @ wo

    a = _A_ref(A_u, A_log_A_real, A_A_imag, A_C_real, A_C_imag, A_log_dt, A_D_skip)
    b = _B_ref(B_x, B_wqkv, B_wo)
    return torch.concatenate([a, b], axis=-1)
```

```cpp
#include <hip/hip_runtime.h>
#include <hip/hip_bf16.h>
typedef __hip_bfloat16 bf16;
constexpr int L=2048, D=512, N=128, H=32, DH=16;
__device__ __forceinline__ float bfr(float x){ return __bfloat162float(__float2bfloat16(x)); }
__global__ void kgen(const bf16* logA,const bf16* Aim,const bf16* Cr,const bf16* Ci,const bf16* logdt,float* K){
  int l=blockIdx.x*256+threadIdx.x, d=blockIdx.y;
  float dt=bfr((float)exp((double)__bfloat162float(logdt[d])));
  float pos=bfr((float)l);
  float acc=0.f;
  for(int n=0;n<N;++n){
    float ar=-bfr(expf(__bfloat162float(logA[d*N+n])))*dt, ai=__bfloat162float(Aim[d*N+n])*dt;
    float cr=__bfloat162float(Cr[d*N+n])*dt, ci=__bfloat162float(Ci[d*N+n])*dt;
    float e=expf(ar*pos), s,c; sincosf(ai*pos,&s,&c);
    acc+=cr*(e*c)-ci*(e*s);
  }
  K[d*L+l]=2.f*acc;
}
__global__ void conv(const bf16* u,const float* K,const bf16* Dsk,bf16* out){
  int d=blockIdx.x*256+threadIdx.x, l=blockIdx.y;
  float acc=0.f;
  for(int j=0;j<=l;++j) acc+=K[d*L+j]*__bfloat162float(u[(l-j)*D+d]);
  float y=bfr(acc);
  float s=bfr(__bfloat162float(u[l*D+d])*__bfloat162float(Dsk[d]));
  out[l*1024+d]=__float2bfloat16(y+s);
}
__global__ void gemm(const bf16* A,const bf16* B,bf16* C,int M,int Nn,int Kk,int ldc){
  int n=blockIdx.x*256+threadIdx.x, m=blockIdx.y;
  float acc=0.f;
  for(int k=0;k<Kk;++k) acc+=__bfloat162float(A[m*Kk+k])*__bfloat162float(B[k*Nn+n]);
  C[m*ldc+n]=__float2bfloat16(acc);
}
__global__ void attn(const bf16* qkv,bf16* o){
  int q=blockIdx.x*256+threadIdx.x, h=blockIdx.y;
  float qv[16]; for(int i=0;i<16;++i) qv[i]=__bfloat162float(qkv[q*1536+h*16+i]);
  float m=-1e30f,lsum=0.f,acc[16]; for(int i=0;i<16;++i)acc[i]=0.f;
  for(int k=0;k<L;++k){
    float s=0.f; for(int i=0;i<16;++i) s+=qv[i]*__bfloat162float(qkv[k*1536+512+h*16+i]);
    s*=0.25f;
    float mn=fmaxf(m,s); float a=expf(m-mn), p=expf(s-mn);
    lsum=lsum*a+p; for(int i=0;i<16;++i) acc[i]=acc[i]*a+p*__bfloat162float(qkv[k*1536+1024+h*16+i]);
    m=mn;
  }
  for(int i=0;i<16;++i) o[q*512+h*16+i]=__float2bfloat16(acc[i]/lsum);
}
extern "C" void kernel_launch(void* const* d_in, const int* in_sizes, int n_in,
                              void* d_out, int out_size, void* d_ws, size_t ws_size,
                              hipStream_t stream) {
  const bf16* u=(const bf16*)d_in[0]; const bf16* logA=(const bf16*)d_in[1]; const bf16* Aim=(const bf16*)d_in[2];
  const bf16* Cr=(const bf16*)d_in[3]; const bf16* Ci=(const bf16*)d_in[4]; const bf16* logdt=(const bf16*)d_in[5];
  const bf16* Dsk=(const bf16*)d_in[6]; const bf16* x=(const bf16*)d_in[7]; const bf16* wqkv=(const bf16*)d_in[8]; const bf16* wo=(const bf16*)d_in[9];
  bf16* out=(bf16*)d_out;
  float* K=(float*)d_ws; bf16* qkv=(bf16*)((char*)d_ws+8*1024*1024); bf16* o=(bf16*)((char*)d_ws+16*1024*1024);
  kgen<<<dim3(L/256,D),256,0,stream>>>(logA,Aim,Cr,Ci,logdt,K);
  conv<<<dim3(D/256,L),256,0,stream>>>(u,K,Dsk,out);
  gemm<<<dim3(1536/256,L),256,0,stream>>>(x,wqkv,qkv,L,1536,512,1536);
  attn<<<dim3(L/256,H),256,0,stream>>>(qkv,o);
  gemm<<<dim3(512/256,L),256,0,stream>>>(o,wo,out+512,L,512,512,1024);
}
```

```cpp
#include <hip/hip_runtime.h>
#include <hip/hip_bf16.h>

#define TID ((int)__builtin_amdgcn_workitem_id_x())
#define BID ((int)__builtin_amdgcn_workgroup_id_x())
#define SYNC() do { __builtin_amdgcn_fence(__ATOMIC_RELEASE, "workgroup"); __builtin_amdgcn_s_barrier(); __builtin_amdgcn_fence(__ATOMIC_ACQUIRE, "workgroup"); } while (0)
#define U2F(x) __builtin_bit_cast(float, (unsigned)(x))
#define F2U(x) __builtin_bit_cast(unsigned, (float)(x))
typedef unsigned short u16;
typedef short bf16x8 __attribute__((ext_vector_type(8)));
typedef short s16x4 __attribute__((ext_vector_type(4)));
typedef float f32x16 __attribute__((ext_vector_type(16)));
typedef float f32x4 __attribute__((ext_vector_type(4)));
typedef float f32x2 __attribute__((ext_vector_type(2)));
typedef unsigned u32x4 __attribute__((ext_vector_type(4)));
typedef unsigned u32x2 __attribute__((ext_vector_type(2)));
typedef __bf16 bf16x2_t __attribute__((ext_vector_type(2)));
typedef __attribute__((address_space(3))) s16x4* lds_s16x4_ptr;
#if defined(__HIP_DEVICE_COMPILE__)
#define NOPK __attribute__((target("no-packed-fp32-ops")))
#else
#define NOPK
#endif
#define DI NOPK __device__ __forceinline__
#define MFMA32(a, b, c) __builtin_amdgcn_mfma_f32_32x32x16_bf16((a), (b), (c), 0, 0, 0)
#define SBAR() __builtin_amdgcn_sched_barrier(0)
#define GLDS16(src, dst) __builtin_amdgcn_global_load_lds((const unsigned*)(src), (unsigned*)(dst), 16, 0, 0)

#define STAMP(i)

constexpr int L = 2048, DM = 512, NS = 128, NH = 32;
constexpr size_t MB = 1024 * 1024;
constexpr size_t WS_Q = 0, WS_K = 2 * MB, WS_V = 4 * MB, WS_QN = 6 * MB + 512 * 1024, WS_KM = 6 * MB + 768 * 1024,
                 WS_O = 7 * MB, WS_WO = 9 * MB, WS_YT = 10 * MB, WS_UR = 12 * MB, WS_KT = 15 * MB;
constexpr int URROW = 4224;
constexpr int VSTEP = 544;
constexpr int VHEAD = 128 * VSTEP;
constexpr float QSCALE = 0.25f * 1.4426950408889634f;
constexpr int UCOPY = 4160, UIMG = 4 * UCOPY, KROW = 80, KIMG = 96 * KROW, CHIMG = UIMG + 2 * KIMG;

DI unsigned pack2(float a, float b) { f32x2 v = {a, b}; bf16x2_t r = __builtin_convertvector(v, bf16x2_t); return __builtin_bit_cast(unsigned, r); }
DI float bf_lo(unsigned p) { return U2F(p << 16); }
DI float bf_hi(unsigned p) { return U2F(p & 0xffff0000u); }
DI float bf2f(u16 v) { return U2F(((unsigned)v) << 16); }
DI float bfr(float x) { return bf_lo(pack2(x, 0.f)); }
DI int crow(int reg, int hh) { return (reg & 3) + 8 * (reg >> 2) + 4 * hh; }
template <int CTRL> DI float dppf(float x) { return __builtin_bit_cast(float, __builtin_amdgcn_update_dpp(0, __builtin_bit_cast(int, x), CTRL, 0xf, 0xf, false)); }
DI float add_other_half(float x) { auto rr = __builtin_amdgcn_permlane32_swap(F2U(x), F2U(x), false, false); return U2F(rr[0]) + U2F(rr[1]); }
DI float max_other_half(float x) { auto rr = __builtin_amdgcn_permlane32_swap(F2U(x), F2U(x), false, false); return __builtin_fmaxf(U2F(rr[0]), U2F(rr[1])); }
DI float row16_max(float x) { x = __builtin_fmaxf(x, dppf<0x128>(x)); x = __builtin_fmaxf(x, dppf<0x124>(x)); x = __builtin_fmaxf(x, dppf<0x122>(x)); x = __builtin_fmaxf(x, dppf<0x121>(x)); return x; }

DI void lds_barrier(unsigned* ctr, unsigned target) {
  const unsigned addr = (unsigned)(size_t)ctr;
  asm volatile("s_waitcnt lgkmcnt(0)" ::: "memory");
  if ((TID & 63) == 0) asm volatile("ds_add_u32 %0, %1" :: "v"(addr), "v"(1u) : "memory");
  for (;;) {
    unsigned v;
    asm volatile("ds_read_b32 %0, %1\n\ts_waitcnt lgkmcnt(0)" : "=v"(v) : "v"(addr) : "memory");
    if ((unsigned)__builtin_amdgcn_readfirstlane(v) >= target) break;
    __builtin_amdgcn_s_sleep(1);
  }
}

struct MapT { u16 v[2048]; };
constexpr MapT make_map() {
  MapT m{};
  for (int l = 0; l < 2048; ++l) {
    int P = l;
    if (l >= 256) {
      const int s = l >= 1024 ? 3 : (l >= 512 ? 2 : 1);
      int q = l >> s; const int rem = l & ((1 << s) - 1), half = 1 << (s - 1);
      if (rem > half || (rem == half && (q & 1))) ++q;
      P = q << s;
    }
    const int rem = P & 127, lo = rem >> 3, f = rem & 7, b = P >> 7;
    int i = 0;
    if (P < 256) i = 8 * b + f;
    else if (P < 512) i = 16 + 4 * (b - 2) + (f >> 1);
    else if (P < 1024) i = 24 + 2 * (b - 4) + (f >> 2);
    else i = 32 + (b - 8);
    m.v[l] = (u16)(i * 16 + lo);
  }
  return m;
}
__device__ const MapT KMAP = make_map();

constexpr int K1_W = 0, K1_X = 36864, K1_AUX = 86016, KG_STRIDE = 132, KG_AREA = 57 * KG_STRIDE * 4, K1_UT = K1_AUX + 2 * KG_AREA, K1_CTR = K1_UT + 64 * 72 * 2, LDS_K1 = K1_CTR + 64;

DI void lds_signal(unsigned addr) {
  if ((TID & 63) == 0) asm volatile("ds_add_u32 %0, %1" :: "v"(addr), "v"(1u) : "memory");
}
DI void lds_wait(unsigned addr, unsigned target) {
  for (;;) {
    unsigned v;
    asm volatile("ds_read_b32 %0, %1\n\ts_waitcnt lgkmcnt(0)" : "=v"(v) : "v"(addr) : "memory");
    if ((unsigned)__builtin_amdgcn_readfirstlane(v) >= target) break;
    __builtin_amdgcn_s_sleep(1);
  }
}

DI void gemm_loader(int b, int mb, int nb, int wave, const u16* __restrict__ x, const u16* __restrict__ w, const u16* __restrict__ u,
                    const u16* __restrict__ wo, char* __restrict__ ws, char* lds) {
  const int lane = TID & 63, gt = TID;
  const int tok0 = mb * 128 + wave * 32;
  const u16* wsrc[3]; const u16* xsrc[4];
#pragma unroll
  for (int i = 0; i < 3; ++i) { const int idx = (wave * 3 + i) * 64 + lane, row = idx / 12, ch = idx - row * 12; wsrc[i] = w + (size_t)row * 1536 + nb * 96 + ch * 8; }
#pragma unroll
  for (int j = 0; j < 4; ++j) { const int idx = j * 64 + lane, row = idx >> 3, ch = (idx & 7) ^ ((row >> 1) & 7); xsrc[j] = x + (size_t)(tok0 + row) * 512 + ch * 8; }
  char* wdst = lds + K1_W + (wave * 3 * 64 + lane) * 16;
  char* xdst = lds + K1_X + wave * 12288 + lane * 16;
  const unsigned cfull = (unsigned)(size_t)(lds + K1_CTR), cdone = cfull + 4;
  const int l0 = (b >> 3) * 64, d0 = (b & 7) * 64;
  int slot = 0;
#pragma nounroll
  for (int c = 0; c < 8; ++c) {
    if (c >= 2) {
      asm volatile("s_waitcnt vmcnt(7)" ::: "memory");
      lds_signal(cfull);
      if (c >= 3) lds_wait(cdone, 4 * (c - 2));
    }
#pragma unroll
    for (int i = 0; i < 3; ++i) { GLDS16(wsrc[i], wdst + slot * 12288 + i * 1024); wsrc[i] += 64 * 1536; }
#pragma unroll
    for (int j = 0; j < 4; ++j) { GLDS16(xsrc[j], xdst + slot * 4096 + j * 1024); xsrc[j] += 64; }
    slot = slot == 2 ? 0 : slot + 1;
  }
  char* ut = lds + K1_UT;
#pragma unroll
  for (int i = 0; i < 2; ++i) { const int idx = gt + 256 * i, row = idx >> 3, ch = (idx & 7) ^ ((row >> 1) & 7); GLDS16(u + (size_t)(l0 + row) * DM + d0 + ch * 8, ut + (wave * 64 + 256 * i) * 16 + lane * 16); }
  asm volatile("s_waitcnt vmcnt(9)" ::: "memory"); lds_signal(cfull);
  asm volatile("s_waitcnt vmcnt(2)" ::: "memory"); lds_signal(cfull);
  if (gt < 128) {
    const int gi = b * 128 + gt;
    const int ln = gi & 63, h = (gi >> 6) & 31, nt = gi >> 11, c = ln & 31, hh = ln >> 5;
    u32x4 v;
#pragma unroll
    for (int j = 0; j < 4; ++j) {
      const int dd = (j < 2 ? 4 * hh + 2 * j : 8 + 4 * hh + 2 * (j - 2));
      v[j] = (unsigned)wo[(size_t)(h * 16 + dd) * DM + nt * 32 + c] | ((unsigned)wo[(size_t)(h * 16 + dd + 1) * DM + nt * 32 + c] << 16);
    }
    *(u32x4*)(ws + WS_WO + (size_t)gi * 16) = v;
  }
  asm volatile("s_waitcnt vmcnt(0)" ::: "memory");
  lds_barrier((unsigned*)(lds + K1_CTR) + 4, 4);
#pragma unroll
  for (int i = 0; i < 2; ++i) {
    const int idx = gt + 256 * i, dd = idx >> 3, gi = idx & 7;
    u32x4 v;
#pragma unroll
    for (int j = 0; j < 4; ++j) {
      const int r1 = 63 - 8 * gi - 2 * j, r0 = r1 - 1;
      const int co = (((dd >> 3) ^ ((r1 >> 1) & 7)) * 8 + (dd & 7)) * 2;
      v[j] = (unsigned)*(const u16*)(ut + r1 * 128 + co) | ((unsigned)*(const u16*)(ut + r0 * 128 + co) << 16);
    }
    *(u32x4*)(ws + WS_UR + (size_t)(d0 + dd) * URROW + (1984 - l0 + 8 * gi) * 2) = v;
    if (l0 == 0) { u32x4 z = {0, 0, 0, 0}; *(u32x4*)(ws + WS_UR + (size_t)(d0 + dd) * URROW + 4096 + gi * 16) = z; }
  }
}

template <int NV>
DI void gemm_consumer(int mb, int nb, int wave, char* __restrict__ ws, char* lds) {
  const int lane = TID & 63, r = lane & 31, hh = lane >> 5;
  const int tok0 = mb * 128 + wave * 32;
  f32x16 acc[3];
#pragma unroll
  for (int t = 0; t < 3; ++t)
#pragma unroll
    for (int i = 0; i < 16; ++i) acc[t][i] = 0.f;
  const int q = (lane & 15) >> 2, p = lane & 3, blk = (lane >> 4) & 1;
  const char* tb = lds + K1_W + (8 * hh + q) * 192 + (16 * blk + 4 * p) * 2;
  const char* xb = lds + K1_X + wave * 12288 + r * 128;
  const int xsw = (r >> 1) & 7;
  const unsigned cfull = (unsigned)(size_t)(lds + K1_CTR), cdone = cfull + 4;
  int slot = 0;
#pragma nounroll
  for (int c = 0; c < 8; ++c) {
    lds_wait(cfull, 4 * (c + 1));
    if (c == 0) STAMP(1);
    const char* wa = tb + slot * 12288;
    const char* xa = xb + slot * 4096;
#pragma unroll
    for (int ks = 0; ks < 4; ++ks) {
      const bf16x8 xf = *(const bf16x8*)(xa + (((2 * ks + hh) ^ xsw) * 16));
#pragma unroll
      for (int t = 0; t < 3; ++t) {
        const char* a = wa + ks * 16 * 192 + t * 64;
        s16x4 lo = __builtin_amdgcn_ds_read_tr16_b64_v4i16((lds_s16x4_ptr)a);
        s16x4 hi = __builtin_amdgcn_ds_read_tr16_b64_v4i16((lds_s16x4_ptr)(a + 4 * 192));
        bf16x8 wf = __builtin_shufflevector(lo, hi, 0, 1, 2, 3, 4, 5, 6, 7);
        if (t >= 3 - NV) acc[t] = MFMA32(xf, wf, acc[t]);
        else acc[t] = MFMA32(wf, xf, acc[t]);
      }
    }
    asm volatile("s_waitcnt lgkmcnt(0)" ::: "memory");
    lds_signal(cdone);
    slot = slot == 2 ? 0 : slot + 1;
  }
  STAMP(2);
  const int tt = tok0 >> 5;
#pragma unroll
  for (int t = 0; t < 3; ++t) {
    const int f0 = nb * 96 + t * 32;
    if (t >= 3 - NV) {
      const int h = ((f0 - 1024) >> 4) + (r >> 4), d = r & 15;
#pragma unroll
      for (int s = 0; s < 2; ++s) {
        u32x4 v;
#pragma unroll
        for (int j = 0; j < 4; ++j) v[j] = pack2(acc[t][8 * s + 2 * j], acc[t][8 * s + 2 * j + 1]);
        char* vb = ws + WS_V + (size_t)h * VHEAD + (2 * tt + s) * VSTEP + hh * 272;
        *(u32x4*)(vb + d * 16) = v;
        if (d == 0) { u32x4 one = {0x3f803f80u, 0x3f803f80u, 0x3f803f80u, 0x3f803f80u}; *(u32x4*)(vb + 256) = one; }
      }
    } else {
      const bool isQ = f0 < 512;
      const float sc = isQ ? QSCALE : 1.f;
#pragma unroll
      for (int a = 0; a < 2; ++a) {
        const int h = ((f0 & 511) >> 4) + a;
        u32x4 v; float n2 = 0.f;
#pragma unroll
        for (int j = 0; j < 4; ++j) {
          v[j] = pack2(acc[t][8 * a + 2 * j] * sc, acc[t][8 * a + 2 * j + 1] * sc);
          const float e0 = bf_lo(v[j]), e1 = bf_hi(v[j]);
          n2 += e0 * e0 + e1 * e1;
        }
        *(u32x4*)(ws + (isQ ? WS_Q : WS_K) + ((size_t)(h * 64 + tt) * 64 + lane) * 16) = v;
        n2 = add_other_half(n2);
        const float nmx = row16_max(n2);
        float* dst = isQ ? (float*)(ws + WS_QN) + h * 2048 + tok0 + r : (float*)(ws + WS_KM) + (h * 64 + tt) * 2 + (lane >> 4);
        if (isQ ? (hh == 0) : ((lane & 47) == 0)) *dst = isQ ? n2 : nmx;
      }
    }
  }
  STAMP(3);
}

struct cf { float r, i; };
DI cf cmul(cf a, cf b) { cf c; c.r = a.r * b.r - a.i * b.i; c.i = a.r * b.i + a.i * b.r; return c; }
__device__ __noinline__ float exp_precise(float x) { return (float)exp((double)x); }
DI float exp_bf16(float x) {
  float e = __builtin_expf(x);
  const int b = (int)(F2U(e) & 0xffffu) - 0x8000;
  if (__builtin_expect(b >= -4 && b <= 4, 0)) e = exp_precise(x);
  return bfr(e);
}
DI cf cexp_pos(float ar, float ai, float p) {
  const float e = __builtin_amdgcn_exp2f(ar * p * 1.4426950408889634f);
  const float y = ai * p;
  const float c_hi = 0.15915494309189535f, c_lo = (float)(0.15915494309189533577 - (double)0.15915494309189535f);
  const float fr = __builtin_fmaf(y, c_lo, __builtin_fmaf(y, c_hi, -__builtin_rintf(y * c_hi)));
  cf o; o.r = e * __builtin_amdgcn_cosf(fr); o.i = e * __builtin_amdgcn_sinf(fr);
  return o;
}
DI void aux_waves(int b, int g, int gw, const u16* u, const u16* logA, const u16* Aim, const u16* Cr, const u16* Ci, const u16* logdt,
                  const u16* wo, char* ws, char* lds) {
  const int lane = TID & 63, gt = gw * 64 + lane;
  const int d = 2 * b + g;
  unsigned* ctr = (unsigned*)(lds + K1_CTR) + 2 + g;
  unsigned nbar = 0;
#define GBAR() do { nbar += 4; lds_barrier(ctr, nbar); } while (0)
  char* area = lds + K1_AUX + g * KG_AREA;
  const u16 p_dt = logdt[d];
  const int po = d * NS + lane;
  const u16 pa0 = logA[po], pa1 = logA[po + 64], pi0 = Aim[po], pi1 = Aim[po + 64], pr0 = Cr[po], pr1 = Cr[po + 64], pc0 = Ci[po], pc1 = Ci[po + 64];
  STAMP(1);
  float* EA = (float*)area + 2 * lane;
  const float dt = exp_bf16(bf2f(p_dt));
  f32x4 acc[3];
#pragma unroll
  for (int m = 0; m < 3; ++m) { acc[m][0] = 0.f; acc[m][1] = 0.f; acc[m][2] = 0.f; acc[m][3] = 0.f; }
  const int i16 = lane & 15, kq = lane >> 4;
#define PUT(i_, v_) do { f32x2 t_ = {(v_).r, (v_).i}; *(f32x2*)(EA + (i_) * KG_STRIDE) = t_; } while (0)
#pragma nounroll
  for (int h = 0; h < 2; ++h) {
    const float ar = -exp_bf16(bf2f(h ? pa1 : pa0)) * dt, ai = bf2f(h ? pi1 : pi0) * dt;
    cf C; C.r = 2.f * bf2f(h ? pr1 : pr0) * dt; C.i = -2.f * bf2f(h ? pc1 : pc0) * dt;
    if (gw == 0) {
      cf G[8]; G[1] = cexp_pos(ar, -ai, 1.f);
      const cf CH = cmul(C, cexp_pos(ar, -ai, 128.f));
      G[2] = cmul(G[1], G[1]); G[3] = cmul(G[2], G[1]); G[4] = cmul(G[2], G[2]); G[5] = cmul(G[4], G[1]); G[6] = cmul(G[4], G[2]); G[7] = cmul(G[4], G[3]);
      PUT(0, C); PUT(8, CH);
#pragma unroll
      for (int i = 1; i < 8; ++i) { cf v = cmul(C, G[i]); PUT(i, v); cf v2 = cmul(CH, G[i]); PUT(8 + i, v2); }
    } else if (gw == 1) {
      const cf z2 = cexp_pos(ar, -ai, 2.f), h1 = cexp_pos(ar, -ai, 128.f);
      cf c2 = cmul(C, cexp_pos(ar, -ai, 256.f)), c4 = cmul(C, cexp_pos(ar, -ai, 512.f));
      const cf z4 = cmul(z2, z2), z6 = cmul(z4, z2);
      cf c3 = cmul(c2, h1);
      PUT(16, c2); { cf v = cmul(c2, z2); PUT(17, v); v = cmul(c2, z4); PUT(18, v); v = cmul(c2, z6); PUT(19, v); }
      PUT(20, c3); { cf v = cmul(c3, z2); PUT(21, v); v = cmul(c3, z4); PUT(22, v); v = cmul(c3, z6); PUT(23, v); }
#pragma unroll
      for (int k = 0; k < 4; ++k) { PUT(24 + 2 * k, c4); cf v = cmul(c4, z4); PUT(25 + 2 * k, v); if (k < 3) c4 = cmul(c4, h1); }
    } else if (gw == 2) {
      const cf h1 = cexp_pos(ar, -ai, 128.f);
      cf c8 = cmul(C, cexp_pos(ar, -ai, 1024.f)), c12 = cmul(C, cexp_pos(ar, -ai, 1536.f));
#pragma unroll
      for (int k = 0; k < 5; ++k) { if (k < 4) { PUT(32 + k, c8); if (k < 3) c8 = cmul(c8, h1); } PUT(36 + k, c12); if (k < 4) c12 = cmul(c12, h1); }
    } else {
      const cf w1 = cexp_pos(ar, ai, 8.f);
      cf F = w1, F8 = cexp_pos(ar, ai, 64.f);
      { cf one; one.r = 1.f; one.i = 0.f; PUT(41, one); }
      PUT(49, F8);
#pragma unroll
      for (int j = 1; j < 8; ++j) { PUT(41 + j, F); F8 = cmul(F8, w1); PUT(49 + j, F8); if (j < 7) F = cmul(F, w1); }
    }
    if (h == 0) STAMP(5);
    GBAR();
#pragma unroll
    for (int gq = 0; gq < 2; ++gq) {
      const float* op = (const float*)area + i16 * KG_STRIDE + gw * 32 + gq * 16 + kq * 4;
      const f32x4 bv = *(const f32x4*)(op + 41 * KG_STRIDE);
#pragma unroll
      for (int m = 0; m < 3; ++m) {
        const f32x4 av = *(const f32x4*)(op + 16 * m * KG_STRIDE);
#pragma unroll
        for (int s = 0; s < 4; ++s) acc[m] = __builtin_amdgcn_mfma_f32_16x16x4f32(av[s], bv[s], acc[m], 0, 0, 0);
      }
    }
    GBAR();
    if (h == 0) STAMP(6);
  }
  STAMP(2);
  float* tab = (float*)area;
#pragma unroll
  for (int m = 0; m < 3; ++m)
#pragma unroll
    for (int j = 0; j < 4; ++j) tab[gw * 768 + (16 * m + 4 * kq + j) * 16 + i16] = acc[m][j];
  GBAR();
  STAMP(3);
#pragma unroll
  for (int i = 0; i < 3; ++i) {
    const int e = gt + 256 * i;
    const float k = (tab[e] + tab[768 + e]) + (tab[1536 + e] + tab[2304 + e]);
    const float hi = bfr(k);
    ((unsigned*)(ws + WS_KT))[d * 768 + e] = pack2(hi, k - hi);
  }
  STAMP(4);
}

NOPK __global__ __launch_bounds__(1024, 4) void k_pre(const u16* __restrict__ u, const u16* __restrict__ logA, const u16* __restrict__ Aim,
                                                const u16* __restrict__ Cr, const u16* __restrict__ Ci, const u16* __restrict__ logdt,
                                                const u16* __restrict__ x, const u16* __restrict__ w, const u16* __restrict__ wo, char* __restrict__ ws) {
  extern __shared__ __attribute__((aligned(16))) char lds[];
  const int wave = __builtin_amdgcn_readfirstlane(TID >> 6), b = BID;
  STAMP(0);
  if (TID < 16) ((unsigned*)(lds + K1_CTR))[TID] = 0u;
  SYNC();
  const int xcd = b & 7, idx = b >> 3;
  const int mb = (xcd & 3) * 4 + (idx & 3), nb = (xcd >> 2) * 8 + (idx >> 2);
  if (wave < 4) {
    gemm_loader(b, mb, nb, wave, x, w, u, wo, ws, lds);
    STAMP(1);
  } else if (wave < 8) {
    if (nb < 10) gemm_consumer<0>(mb, nb, wave - 4, ws, lds);
    else if (nb == 10) gemm_consumer<1>(mb, nb, wave - 4, ws, lds);
    else gemm_consumer<3>(mb, nb, wave - 4, ws, lds);
  } else {
    aux_waves(b, (wave - 8) >> 2, (wave - 8) & 3, u, logA, Aim, Cr, Ci, logdt, wo, ws, lds);
  }
}

constexpr int LDS_CP = 2 * CHIMG;
constexpr int LDS_AP = LDS_CP + 4 * 2304 * 4;
constexpr int LDS_FL = LDS_AP + 4 * 2 * 9 * 64 * 4;
constexpr int LDS_K2 = LDS_FL + 64;

#define GLOAD(dst, ptr, off) asm volatile("global_load_dwordx4 %0, %1, off offset:" #off : "=v"(dst) : "v"(ptr))
DI void exp_pack(const f32x16& s, bf16x8& pa, bf16x8& pb) {
  u32x4 a, b;
#pragma unroll
  for (int j = 0; j < 4; ++j) a[j] = pack2(__builtin_amdgcn_exp2f(s[2 * j]), __builtin_amdgcn_exp2f(s[2 * j + 1]));
#pragma unroll
  for (int j = 0; j < 4; ++j) b[j] = pack2(__builtin_amdgcn_exp2f(s[8 + 2 * j]), __builtin_amdgcn_exp2f(s[8 + 2 * j + 1]));
  pa = __builtin_bit_cast(bf16x8, a); pb = __builtin_bit_cast(bf16x8, b);
}
DI void attn_step(const char*& kp, const char*& vp, bf16x8& kc, bf16x8& kn, bf16x8& vca, bf16x8& vcb, bf16x8& vna, bf16x8& vnb,
                  bf16x8 q0, bf16x8 q1, const f32x16& nm0, const f32x16& nm1, f32x16& s0, f32x16& o0, f32x16& o1) {
  bf16x8 pa, pb;
  f32x16 s1 = MFMA32(kc, q1, nm1);
  SBAR();
  GLOAD(vna, vp, 1088); GLOAD(vnb, vp, 1632);
  GLOAD(kc, kp, 2048);
  kp += 1024; vp += 2 * VSTEP;
  SBAR();
  exp_pack(s0, pa, pb);
  asm volatile("s_waitcnt vmcnt(3)" : "+v"(vca), "+v"(vcb), "+v"(kn));
  SBAR();
  o0 = MFMA32(vca, pa, o0);
  o0 = MFMA32(vcb, pb, o0);
  s0 = MFMA32(kn, q0, nm0);
  SBAR();
  exp_pack(s1, pa, pb);
  SBAR();
  o1 = MFMA32(vca, pa, o1);
  o1 = MFMA32(vcb, pb, o1);
  SBAR();
}
DI void attn_main(const char* kp, const char* vp, bf16x8 q0, bf16x8 q1, const f32x16& nm0, const f32x16& nm1, f32x16& o0, f32x16& o1, int kh) {
#pragma unroll
  for (int i = 0; i < 16; ++i) { o0[i] = 0.f; o1[i] = 0.f; }
  bf16x8 kA, kB, vAa, vAb, vBa, vBb;
  asm volatile("" : "+v"(q0), "+v"(q1));
  GLOAD(kA, kp, 0); GLOAD(vAa, vp, 0); GLOAD(vAb, vp, 544); GLOAD(kB, kp, 1024);
  asm volatile("s_waitcnt vmcnt(3)" : "+v"(kA));
  f32x16 s0 = MFMA32(kA, q0, nm0);
  for (int it = 0; it < 16; ++it) {
    attn_step(kp, vp, kA, kB, vAa, vAb, vBa, vBb, q0, q1, nm0, nm1, s0, o0, o1);
    attn_step(kp, vp, kB, kA, vBa, vBb, vAa, vAb, q0, q1, nm0, nm1, s0, o0, o1);
  }
  asm volatile("s_waitcnt vmcnt(0)" : "+v"(kA), "+v"(kB), "+v"(vAa), "+v"(vAb), "+v"(vBa), "+v"(vBb));
}

NOPK __global__ __launch_bounds__(768, 3) void k_attn_conv(char* __restrict__ ws) {
  extern __shared__ __attribute__((aligned(16))) char lds[];
  const int tid = TID, wave = __builtin_amdgcn_readfirstlane(tid >> 6), lane = tid & 63, r = lane & 31, hh = lane >> 5;
  const int b = BID;
  const int h = (b & 7) * 4 + ((b >> 3) & 3), qt = b >> 5;
#define STAMP2(i)
  STAMP2(0);
  const int qg = wave & 3, kh = (wave >> 2) & 1;
  bf16x8 q0, q1; f32x16 nm0, nm1, o0, o1;
  const char* kp = ws + WS_K + ((size_t)(h * 64 + kh * 32) * 64 + lane) * 16;
  const char* vp = ws + WS_V + (size_t)h * VHEAD + (size_t)(kh * 64) * VSTEP + hh * 272 + (r < 16 ? r : 16) * 16;
  const int qtile = qt * 8 + qg * 2;
  const int cw = wave - 8, cch = cw >> 1, chalf = cw & 1;
  if (tid < 16) ((unsigned*)(lds + LDS_FL))[tid] = 0u;
  SYNC();
  if (wave < 8) {
    q0 = *(const bf16x8*)(ws + WS_Q + ((size_t)(h * 64 + qtile) * 64 + lane) * 16);
    q1 = *(const bf16x8*)(ws + WS_Q + ((size_t)(h * 64 + qtile + 1) * 64 + lane) * 16);
    const f32x2 kmv = ((const f32x2*)(ws + WS_KM))[h * 64 + lane];
    const float n0 = ((const float*)(ws + WS_QN))[h * 2048 + qtile * 32 + r], n1 = ((const float*)(ws + WS_QN))[h * 2048 + qtile * 32 + 32 + r];
    float km = row16_max(__builtin_fmaxf(kmv[0], kmv[1]));
    km = __builtin_fmaxf(__builtin_fmaxf(__builtin_bit_cast(float, __builtin_amdgcn_readlane(__builtin_bit_cast(int, km), 0)), __builtin_bit_cast(float, __builtin_amdgcn_readlane(__builtin_bit_cast(int, km), 16))),
               __builtin_fmaxf(__builtin_bit_cast(float, __builtin_amdgcn_readlane(__builtin_bit_cast(int, km), 32)), __builtin_bit_cast(float, __builtin_amdgcn_readlane(__builtin_bit_cast(int, km), 48))));
    const float m0 = __builtin_amdgcn_sqrtf(n0 * km), m1 = __builtin_amdgcn_sqrtf(n1 * km);
#pragma unroll
    for (int i = 0; i < 16; ++i) { nm0[i] = -m0; nm1[i] = -m1; }
  } else {
    const int ct = chalf * 64 + lane;
    char* img = lds + cch * CHIMG;
    unsigned* tabl = (unsigned*)(lds + LDS_CP) + cch * 2 * 2304;
    const char* ur = ws + WS_UR + (size_t)(2 * b + cch) * URROW;
    const u32x4* kt = (const u32x4*)(ws + WS_KT + (size_t)(2 * b + cch) * 3072);
    u32x4 tv0 = kt[ct], tv1 = {0, 0, 0, 0};
    if (ct < 64) tv1 = kt[128 + ct];
    u32x4 ua[3]; u32x2 ub[3]; u32x4 mp[2];
#pragma unroll
    for (int i = 0; i < 3; ++i) {
      const int g = ct + 128 * i;
      if (g < 260) { ua[i] = *(const u32x4*)(ur + g * 16); ub[i] = *(const u32x2*)(ur + g * 16 + 16); }
    }
#pragma unroll
    for (int i = 0; i < 2; ++i) mp[i] = *(const u32x4*)(KMAP.v + (ct + 128 * i) * 8);
    *(u32x4*)(tabl + ct * 4) = tv0;
    if (ct < 64) *(u32x4*)(tabl + 512 + ct * 4) = tv1;
    { u32x4 z = {0, 0, 0, 0};
      *(u32x4*)(img + UIMG + ct * 16) = z; *(u32x4*)(img + UIMG + KIMG + ct * 16) = z;
      if (ct < 32) { *(u32x4*)(img + UIMG + 2048 + ct * 16) = z; *(u32x4*)(img + UIMG + KIMG + 2048 + ct * 16) = z; } }
#pragma unroll
    for (int i = 0; i < 3; ++i) {
      const int g = ct + 128 * i;
      if (g < 260) {
        const unsigned a0 = __builtin_amdgcn_alignbit(ua[i][1], ua[i][0], 16), a1 = __builtin_amdgcn_alignbit(ua[i][2], ua[i][1], 16),
                       a2 = __builtin_amdgcn_alignbit(ua[i][3], ua[i][2], 16), a3 = __builtin_amdgcn_alignbit(ub[i][0], ua[i][3], 16),
                       a4 = __builtin_amdgcn_alignbit(ub[i][1], ub[i][0], 16);
        const u32x4 c1 = {a0, a1, a2, a3}, c2 = {ua[i][1], ua[i][2], ua[i][3], ub[i][0]}, c3 = {a1, a2, a3, a4};
        *(u32x4*)(img + g * 16) = ua[i]; *(u32x4*)(img + UCOPY + g * 16) = c1;
        *(u32x4*)(img + 2 * UCOPY + g * 16) = c2; *(u32x4*)(img + 3 * UCOPY + g * 16) = c3;
      }
    }
    lds_barrier((unsigned*)(lds + LDS_FL) + 8 + cch, 2);
#pragma unroll
    for (int i = 0; i < 2; ++i) {
      const int g = ct + 128 * i;
      u32x4 vh, vl;
#pragma unroll
      for (int j = 0; j < 4; ++j) {
        const unsigned e0 = tabl[mp[i][j] & 0xffff], e1 = tabl[mp[i][j] >> 16];
        vh[j] = __builtin_amdgcn_perm(e1, e0, 0x05040100u);
        vl[j] = __builtin_amdgcn_perm(e1, e0, 0x07060302u);
      }
      *(u32x4*)(img + UIMG + (32 + (g >> 2)) * KROW + (g & 3) * 16) = vh;
      *(u32x4*)(img + UIMG + KIMG + (32 + (g >> 2)) * KROW + (g & 3) * 16) = vl;
    }
    lds_barrier((unsigned*)(lds + LDS_FL) + 8 + cch, 4);
  }
  STAMP2(1);
  float* ap = (float*)(lds + LDS_AP);
  volatile int* fl = (volatile int*)(lds + LDS_FL);
  for (int pass = 0;; ++pass) {
    if (wave < 8) {
      attn_main(kp, vp, q0, q1, nm0, nm1, o0, o1, kh);
      if (kh == 1) {
#pragma unroll
        for (int i = 0; i < 9; ++i) { ap[((qg * 2 + 0) * 9 + i) * 64 + lane] = o0[i]; ap[((qg * 2 + 1) * 9 + i) * 64 + lane] = o1[i]; }
      }
      const bool bad = !(o0[8] >= 8.2718061e-25f) || !(o1[8] >= 8.2718061e-25f);
      const int anybad = (__builtin_amdgcn_ballot_w64(bad) != 0);
      if (lane == 0) fl[wave] = anybad;
    } else if (pass == 0) {
      const int c = (3 - r) & 3;
      const char* ua = lds + cch * CHIMG + c * UCOPY + 2 * (2047 + 8 * hh - r - c);
      const char* kb = lds + cch * CHIMG + UIMG + (r + 32) * KROW + 16 * hh;
      f32x16 a0, a1;
#pragma unroll
      for (int i = 0; i < 16; ++i) { a0[i] = 0.f; a1[i] = 0.f; }
#pragma unroll 2
      for (int dd = 0; dd < 16; ++dd) {
        const int dl = chalf * 16 + dd;
#pragma unroll
        for (int s = 0; s < 2; ++s) {
          const char* up = ua - 64 * dl + 32 * s;
          const s16x4 x0 = *(const s16x4*)up, x1 = *(const s16x4*)(up + 8);
          const s16x4 y0 = *(const s16x4*)(up - 2048), y1 = *(const s16x4*)(up - 2048 + 8);
          const bf16x8 A0 = __builtin_shufflevector(x0, x1, 0, 1, 2, 3, 4, 5, 6, 7), A1 = __builtin_shufflevector(y0, y1, 0, 1, 2, 3, 4, 5, 6, 7);
          const char* kq = kb - KROW * dl + 32 * s;
          const bf16x8 B0h = *(const bf16x8*)kq, B0l = *(const bf16x8*)(kq + KIMG);
          const bf16x8 B1h = *(const bf16x8*)(kq + 32 * KROW), B1l = *(const bf16x8*)(kq + 32 * KROW + KIMG);
          a0 = MFMA32(A0, B0h, a0); a1 = MFMA32(A0, B1h, a1);
          a0 = MFMA32(A0, B0l, a0); a1 = MFMA32(A0, B1l, a1);
          a1 = MFMA32(A1, B0h, a1); a1 = MFMA32(A1, B0l, a1);
        }
      }
      float* cp = (float*)(lds + LDS_CP) + cw * 2304;
#pragma unroll
      for (int g = 0; g < 4; ++g) {
        f32x4 v0 = {a0[4 * g], a0[4 * g + 1], a0[4 * g + 2], a0[4 * g + 3]}, v1 = {a1[4 * g], a1[4 * g + 1], a1[4 * g + 2], a1[4 * g + 3]};
        *(f32x4*)(cp + 36 * r + 8 * g + 4 * hh) = v0;
        *(f32x4*)(cp + 36 * (32 + r) + 8 * g + 4 * hh) = v1;
      }
      lds_barrier((unsigned*)(lds + LDS_FL) + 8 + cch, 6);
#pragma unroll
      for (int i = 0; i < 2; ++i) {
        const int g = chalf * 128 + i * 64 + lane;
        const float* c0 = (const float*)(lds + LDS_CP) + (2 * cch) * 2304 + 36 * (g >> 2) + 8 * (g & 3);
        const f32x4 a = *(const f32x4*)c0 + *(const f32x4*)(c0 + 2304), bq = *(const f32x4*)(c0 + 4) + *(const f32x4*)(c0 + 2304 + 4);
        u32x4 w = {pack2(a[0], a[1]), pack2(a[2], a[3]), pack2(bq[0], bq[1]), pack2(bq[2], bq[3])};
        *(u32x4*)(ws + WS_YT + ((size_t)(2 * b + cch) * L + 8 * g) * 2) = w;
      }
    }
    STAMP2(2);
    SYNC();
    STAMP2(3);
    int redo = 0;
    if (pass == 0) {
#pragma unroll
      for (int i = 0; i < 8; ++i) redo |= fl[i];
    }
    if (!redo) break;
    if (wave < 8) {
      const char* kall = ws + WS_K + ((size_t)(h * 64) * 64 + lane) * 16;
      float m0 = -3.0e38f, m1 = -3.0e38f;
      f32x16 z;
#pragma unroll
      for (int i = 0; i < 16; ++i) z[i] = 0.f;
      for (int it = 0; it < 64; ++it) {
        const bf16x8 kf = *(const bf16x8*)(kall + it * 1024);
        const f32x16 s0 = MFMA32(kf, q0, z), s1 = MFMA32(kf, q1, z);
#pragma unroll
        for (int i = 0; i < 16; ++i) { m0 = __builtin_fmaxf(m0, s0[i]); m1 = __builtin_fmaxf(m1, s1[i]); }
      }
      m0 = max_other_half(m0); m1 = max_other_half(m1);
#pragma unroll
      for (int i = 0; i < 16; ++i) { nm0[i] = -m0; nm1[i] = -m1; }
    }
    SYNC();
  }
  if (wave < 4) {
#pragma unroll
    for (int t = 0; t < 2; ++t) {
      float v[9];
#pragma unroll
      for (int i = 0; i < 9; ++i) v[i] = (t ? o1[i] : o0[i]) + ap[((qg * 2 + t) * 9 + i) * 64 + lane];
      const float inv = 1.f / v[8];
      u32x4 w;
#pragma unroll
      for (int j = 0; j < 4; ++j) w[j] = pack2(v[2 * j] * inv, v[2 * j + 1] * inv);
      *(u32x4*)(ws + WS_O + ((size_t)((qtile + t) * 32 + h) * 64 + lane) * 16) = w;
    }
  }
  STAMP2(4);
}

constexpr int RED_STRIDE = 68;
DI void out_role(int b, const char* ws, u16* out, char* lds) {
  const int tid = TID, wave = tid >> 6, lane = tid & 63, r = lane & 31, hh = lane >> 5;
  const int xcd = b & 7, idx = b >> 3, mt = xcd * 4 + (idx & 3), nt = idx >> 2;
  bf16x8 of[2][8], wf[2][8];
#pragma unroll
  for (int k = 0; k < 8; ++k) {
    const int h = wave * 8 + k;
#pragma unroll
    for (int i = 0; i < 2; ++i) {
      of[i][k] = *(const bf16x8*)(ws + WS_O + ((size_t)((2 * mt + i) * 32 + h) * 64 + lane) * 16);
      wf[i][k] = *(const bf16x8*)(ws + WS_WO + ((size_t)((2 * nt + i) * 32 + h) * 64 + lane) * 16);
    }
  }
  f32x16 acc[2][2];
#pragma unroll
  for (int i = 0; i < 2; ++i)
#pragma unroll
    for (int j = 0; j < 2; ++j)
#pragma unroll
      for (int e = 0; e < 16; ++e) acc[i][j][e] = 0.f;
#pragma unroll
  for (int k = 0; k < 8; ++k)
#pragma unroll
    for (int i = 0; i < 2; ++i)
#pragma unroll
      for (int j = 0; j < 2; ++j) acc[i][j] = MFMA32(of[i][k], wf[j][k], acc[i][j]);
  float* red = (float*)lds + wave * 64 * RED_STRIDE;
#pragma unroll
  for (int i = 0; i < 2; ++i)
#pragma unroll
    for (int j = 0; j < 2; ++j)
#pragma unroll
      for (int e = 0; e < 16; ++e) red[(32 * i + crow(e, hh)) * RED_STRIDE + 32 * j + r] = acc[i][j][e];
  SYNC();
  const int tok = tid >> 2, n0 = (tid & 3) * 16;
  const float* rp = (const float*)lds + tok * RED_STRIDE + n0;
  u32x4 w[2];
#pragma unroll
  for (int g = 0; g < 4; ++g) {
    f32x4 s = *(const f32x4*)(rp + 4 * g);
#pragma unroll
    for (int wv = 1; wv < 4; ++wv) s += *(const f32x4*)(rp + wv * 64 * RED_STRIDE + 4 * g);
    w[g >> 1][(g & 1) * 2] = pack2(s[0], s[1]); w[g >> 1][(g & 1) * 2 + 1] = pack2(s[2], s[3]);
  }
  u16* op = out + (size_t)(mt * 64 + tok) * 1024 + 512 + nt * 64 + n0;
  *(u32x4*)op = w[0]; *(u32x4*)(op + 8) = w[1];
}
DI void fin_role(int b, const char* ws, const u16* u, const u16* dsk, u16* out, char* lds) {
  const int tid = TID;
  const int l0 = (b >> 3) * 64, d0 = (b & 7) * 64;
  u16* t = (u16*)lds;
#pragma unroll
  for (int i = 0; i < 2; ++i) {
    const int idx = tid + 256 * i, row = idx >> 3, ch = idx & 7;
    *(u32x4*)(t + row * 72 + ch * 8) = *(const u32x4*)(ws + WS_YT + ((size_t)(d0 + row) * L + l0 + ch * 8) * 2);
  }
  SYNC();
#pragma unroll
  for (int i = 0; i < 2; ++i) {
    const int idx = tid + 256 * i, ll = idx >> 3, ch = idx & 7;
    const u32x4 uv = *(const u32x4*)(u + (size_t)(l0 + ll) * DM + d0 + ch * 8);
    const u32x4 dv = *(const u32x4*)(dsk + d0 + ch * 8);
    u32x4 v;
#pragma unroll
    for (int j = 0; j < 4; ++j) {
      const float y0 = bf2f(t[(ch * 8 + 2 * j) * 72 + ll]), y1 = bf2f(t[(ch * 8 + 2 * j + 1) * 72 + ll]);
      const unsigned sk = pack2(bf_lo(uv[j]) * bf_lo(dv[j]), bf_hi(uv[j]) * bf_hi(dv[j]));
      v[j] = pack2(y0 + bf_lo(sk), y1 + bf_hi(sk));
    }
    *(u32x4*)(out + (size_t)(l0 + ll) * 1024 + d0 + ch * 8) = v;
  }
}
NOPK __global__ __launch_bounds__(256) void k_out(const char* ws, const u16* u, const u16* dsk, u16* out) {
  extern __shared__ __attribute__((aligned(16))) char lds[];
  if (BID < 256) out_role(BID, ws, out, lds);
  else fin_role(BID - 256, ws, u, dsk, out, lds);
}

extern "C" void kernel_launch(void* const* d_in, const int* in_sizes, int n_in, void* d_out, int out_size, void* d_ws, size_t ws_size,
                              hipStream_t stream) {
  const u16* u = (const u16*)d_in[0]; const u16* logA = (const u16*)d_in[1]; const u16* Aim = (const u16*)d_in[2];
  const u16* Cr = (const u16*)d_in[3]; const u16* Ci = (const u16*)d_in[4]; const u16* logdt = (const u16*)d_in[5];
  const u16* dsk = (const u16*)d_in[6]; const u16* x = (const u16*)d_in[7]; const u16* wqkv = (const u16*)d_in[8]; const u16* wo = (const u16*)d_in[9];
  u16* out = (u16*)d_out; char* ws = (char*)d_ws;
  constexpr int LDS_OUT = 4 * 64 * RED_STRIDE * 4;
  (void)hipFuncSetAttribute((const void*)k_pre, hipFuncAttributeMaxDynamicSharedMemorySize, LDS_K1);
  (void)hipFuncSetAttribute((const void*)k_attn_conv, hipFuncAttributeMaxDynamicSharedMemorySize, LDS_K2);
  (void)hipFuncSetAttribute((const void*)k_out, hipFuncAttributeMaxDynamicSharedMemorySize, LDS_OUT);
  k_pre<<<256, 1024, LDS_K1, stream>>>(u, logA, Aim, Cr, Ci, logdt, x, wqkv, wo, ws);
  k_attn_conv<<<256, 768, LDS_K2, stream>>>(ws);
  k_out<<<512, 256, LDS_OUT, stream>>>(ws, u, dsk, out);
}
```

```cpp
#include <hip/hip_runtime.h>
#include <hip/hip_bf16.h>

#define TID ((int)__builtin_amdgcn_workitem_id_x())
#define BID ((int)__builtin_amdgcn_workgroup_id_x())
#define SYNC() do { __builtin_amdgcn_fence(__ATOMIC_RELEASE, "workgroup"); __builtin_amdgcn_s_barrier(); __builtin_amdgcn_fence(__ATOMIC_ACQUIRE, "workgroup"); } while (0)
#define UMAX(a, b) ((a) > (b) ? (a) : (b))
#define U2F(x) __builtin_bit_cast(float, (unsigned)(x))
#define F2U(x) __builtin_bit_cast(unsigned, (float)(x))
typedef unsigned short u16;
typedef short bf16x8 __attribute__((ext_vector_type(8)));
typedef short s16x4 __attribute__((ext_vector_type(4)));
typedef float f32x16 __attribute__((ext_vector_type(16)));
typedef float f32x4 __attribute__((ext_vector_type(4)));
typedef float f32x2 __attribute__((ext_vector_type(2)));
typedef unsigned u32x4 __attribute__((ext_vector_type(4)));
typedef unsigned u32x2 __attribute__((ext_vector_type(2)));
typedef __bf16 bf16x2_t __attribute__((ext_vector_type(2)));
typedef _Float16 f16x8 __attribute__((ext_vector_type(8)));
#define MFMA32H(a, b, c) __builtin_amdgcn_mfma_f32_32x32x16_f16(__builtin_bit_cast(f16x8, a), __builtin_bit_cast(f16x8, b), (c), 0, 0, 0)
typedef __attribute__((address_space(3))) s16x4* lds_s16x4_ptr;
#if defined(__HIP_DEVICE_COMPILE__)
#define NOPK __attribute__((target("no-packed-fp32-ops")))
#else
#define NOPK
#endif
#define DI NOPK __device__ __forceinline__
#define MFMA32(a, b, c) __builtin_amdgcn_mfma_f32_32x32x16_bf16((a), (b), (c), 0, 0, 0)
#define SBAR() __builtin_amdgcn_sched_barrier(0)
#define GLDS16(src, dst) __builtin_amdgcn_global_load_lds((const unsigned*)(src), (unsigned*)(dst), 16, 0, 0)

#define STAMP(i)

constexpr int L = 2048, DM = 512, NS = 128, NH = 32;
constexpr size_t MB = 1024 * 1024;
constexpr size_t WS_Q = 0, WS_K = 2 * MB, WS_V = 4 * MB, WS_QN = 6 * MB + 512 * 1024, WS_KM = 6 * MB + 768 * 1024,
                 WS_O = 7 * MB, WS_WO = 9 * MB, WS_YT = 10 * MB, WS_UR = 12 * MB, WS_KT = 15 * MB;
constexpr int URROW = 4224;
constexpr int VSTEP = 576;
constexpr int VHEAD = 128 * VSTEP;
constexpr float QSCALE = 0.25f * 1.4426950408889634f;
constexpr int UCOPY = 4160, UIMG = 4 * UCOPY, KROW = 80, KIMG = 96 * KROW, CHIMG = UIMG + KIMG;

#define GSTAMP(k, i, wsp)
DI unsigned pack2(float a, float b) { f32x2 v = {a, b}; bf16x2_t r = __builtin_convertvector(v, bf16x2_t); return __builtin_bit_cast(unsigned, r); }
DI void st16(void* p, u32x4 v) { asm volatile("global_store_dwordx4 %0, %1, off sc1\n\ts_nop 1" :: "v"(p), "v"(v) : "memory"); }
DI void st4(void* p, unsigned v) { asm volatile("global_store_dword %0, %1, off sc1" :: "v"(p), "v"(v) : "memory"); }
DI float bf_lo(unsigned p) { return U2F(p << 16); }
DI float bf_hi(unsigned p) { return U2F(p & 0xffff0000u); }
DI float bf2f(u16 v) { return U2F(((unsigned)v) << 16); }
DI float bfr(float x) { return bf_lo(pack2(x, 0.f)); }
DI int crow(int reg, int hh) { return (reg & 3) + 8 * (reg >> 2) + 4 * hh; }
template <int CTRL> DI float dppf(float x) { return __builtin_bit_cast(float, __builtin_amdgcn_update_dpp(0, __builtin_bit_cast(int, x), CTRL, 0xf, 0xf, false)); }
DI float add_other_half(float x) { auto rr = __builtin_amdgcn_permlane32_swap(F2U(x), F2U(x), false, false); return U2F(rr[0]) + U2F(rr[1]); }
DI float max_other_half(float x) { auto rr = __builtin_amdgcn_permlane32_swap(F2U(x), F2U(x), false, false); return __builtin_fmaxf(U2F(rr[0]), U2F(rr[1])); }
DI float row16_max(float x) { x = __builtin_fmaxf(x, dppf<0x128>(x)); x = __builtin_fmaxf(x, dppf<0x124>(x)); x = __builtin_fmaxf(x, dppf<0x122>(x)); x = __builtin_fmaxf(x, dppf<0x121>(x)); return x; }

DI void lds_barrier(unsigned* ctr, unsigned target) {
  const unsigned addr = (unsigned)(size_t)ctr;
  asm volatile("s_waitcnt lgkmcnt(0)" ::: "memory");
  if ((TID & 63) == 0) asm volatile("ds_add_u32 %0, %1" :: "v"(addr), "v"(1u) : "memory");
  for (;;) {
    unsigned v;
    asm volatile("ds_read_b32 %0, %1\n\ts_waitcnt lgkmcnt(0)" : "=v"(v) : "v"(addr) : "memory");
    if ((unsigned)__builtin_amdgcn_readfirstlane(v) >= target) break;
    __builtin_amdgcn_s_sleep(1);
  }
}

struct MapT { u16 v[2048]; };
constexpr MapT make_map() {
  MapT m{};
  for (int l = 0; l < 2048; ++l) {
    int P = l;
    if (l >= 256) {
      const int s = l >= 1024 ? 3 : (l >= 512 ? 2 : 1);
      int q = l >> s; const int rem = l & ((1 << s) - 1), half = 1 << (s - 1);
      if (rem > half || (rem == half && (q & 1))) ++q;
      P = q << s;
    }
    const int rem = P & 127, lo = rem >> 3, f = rem & 7, b = P >> 7;
    int i = 0;
    if (P < 256) i = 8 * b + f;
    else if (P < 512) i = 16 + 4 * (b - 2) + (f >> 1);
    else if (P < 1024) i = 24 + 2 * (b - 4) + (f >> 2);
    else i = 32 + (b - 8);
    m.v[l] = (u16)(i * 16 + lo);
  }
  return m;
}
__device__ const MapT KMAP = make_map();

constexpr int K1_W = 0, K1_X = 36864, K1_AUX = 86016, KG_ROW = 544, KG_AREA = 57 * KG_ROW, K1_UT = K1_AUX + 2 * KG_AREA, K1_CTR = K1_UT + 64 * 72 * 2, LDS_K1 = K1_CTR + 128;

DI void lds_signal(unsigned addr) {
  if ((TID & 63) == 0) asm volatile("ds_add_u32 %0, %1" :: "v"(addr), "v"(1u) : "memory");
}
DI void lds_wait(unsigned addr, unsigned target) {
  for (;;) {
    unsigned v;
    asm volatile("ds_read_b32 %0, %1\n\ts_waitcnt lgkmcnt(0)" : "=v"(v) : "v"(addr) : "memory");
    if ((unsigned)__builtin_amdgcn_readfirstlane(v) >= target) break;
    __builtin_amdgcn_s_sleep(1);
  }
}

DI void gemm_loader(int b, int mb, int nb, int wave, const u16* __restrict__ x, const u16* __restrict__ w, const u16* __restrict__ u,
                    const u16* __restrict__ wo, char* __restrict__ ws, char* lds) {
  const int lane = TID & 63, gt = TID;
  const int tok0 = mb * 128 + wave * 32;
  const u16* wsrc[3]; const u16* xsrc[4];
#pragma unroll
  for (int i = 0; i < 3; ++i) { const int idx = (wave * 3 + i) * 64 + lane, row = idx / 12, ch = idx - row * 12; wsrc[i] = w + (size_t)row * 1536 + nb * 96 + ch * 8; }
#pragma unroll
  for (int j = 0; j < 4; ++j) { const int idx = j * 64 + lane, row = idx >> 3, ch = (idx & 7) ^ ((row >> 1) & 7); xsrc[j] = x + (size_t)(tok0 + row) * 512 + ch * 8; }
  char* wdst = lds + K1_W + (wave * 3 * 64 + lane) * 16;
  char* xdst = lds + K1_X + wave * 12288 + lane * 16;
  const unsigned cfull = (unsigned)(size_t)(lds + K1_CTR), cdone = cfull + 32;
  const int l0 = (b >> 3) * 64, d0 = (b & 7) * 64;
  int slot = 0;
#pragma nounroll
  for (int c = 0; c < 8; ++c) {
    if (c >= 2) {
      asm volatile("s_waitcnt vmcnt(7)" ::: "memory");
      lds_signal(cfull + 4 * (c - 2));
      if (c >= 3) lds_wait(cdone + 4 * (c - 3), 4);
    }
#pragma unroll
    for (int i = 0; i < 3; ++i) { GLDS16(wsrc[i], wdst + slot * 12288 + i * 1024); wsrc[i] += 64 * 1536; }
#pragma unroll
    for (int j = 0; j < 4; ++j) { GLDS16(xsrc[j], xdst + slot * 4096 + j * 1024); xsrc[j] += 64; }
    slot = slot == 2 ? 0 : slot + 1;
  }
  char* ut = lds + K1_UT;
#pragma unroll
  for (int i = 0; i < 2; ++i) { const int idx = gt + 256 * i, row = idx >> 3, ch = (idx & 7) ^ ((row >> 1) & 7); GLDS16(u + (size_t)(l0 + row) * DM + d0 + ch * 8, ut + (wave * 64 + 256 * i) * 16 + lane * 16); }
  asm volatile("s_waitcnt vmcnt(9)" ::: "memory"); lds_signal(cfull + 24);
  asm volatile("s_waitcnt vmcnt(2)" ::: "memory"); lds_signal(cfull + 28);
  if (gt < 128) {
    const int gi = b * 128 + gt;
    const int ln = gi & 63, h = (gi >> 6) & 31, nt = gi >> 11, c = ln & 31, hh = ln >> 5;
    u32x4 v;
#pragma unroll
    for (int j = 0; j < 4; ++j) {
      const int dd = (j < 2 ? 4 * hh + 2 * j : 8 + 4 * hh + 2 * (j - 2));
      v[j] = (unsigned)wo[(size_t)(h * 16 + dd) * DM + nt * 32 + c] | ((unsigned)wo[(size_t)(h * 16 + dd + 1) * DM + nt * 32 + c] << 16);
    }
    st16(ws + WS_WO + (size_t)gi * 16, v);
  }
  asm volatile("s_waitcnt vmcnt(0)" ::: "memory");
  lds_barrier((unsigned*)(lds + K1_CTR) + 18, 4);
#pragma unroll
  for (int i = 0; i < 2; ++i) {
    const int idx = gt + 256 * i, dd = idx >> 3, gi = idx & 7;
    u32x4 v;
#pragma unroll
    for (int j = 0; j < 4; ++j) {
      const int r1 = 63 - 8 * gi - 2 * j, r0 = r1 - 1;
      const int co = (((dd >> 3) ^ ((r1 >> 1) & 7)) * 8 + (dd & 7)) * 2;
      v[j] = (unsigned)*(const u16*)(ut + r1 * 128 + co) | ((unsigned)*(const u16*)(ut + r0 * 128 + co) << 16);
    }
    st16(ws + WS_UR + (size_t)(d0 + dd) * URROW + (1984 - l0 + 8 * gi) * 2, v);
    if (l0 == 0) { u32x4 z = {0, 0, 0, 0}; st16(ws + WS_UR + (size_t)(d0 + dd) * URROW + 4096 + gi * 16, z); }
  }
}

template <int NV>
DI void gemm_consumer(int mb, int nb, int wave, char* __restrict__ ws, char* lds) {
  const int lane = TID & 63, r = lane & 31, hh = lane >> 5;
  const int tok0 = mb * 128 + wave * 32;
  f32x16 acc[3];
#pragma unroll
  for (int t = 0; t < 3; ++t)
#pragma unroll
    for (int i = 0; i < 16; ++i) acc[t][i] = 0.f;
  const int q = (lane & 15) >> 2, p = lane & 3, blk = (lane >> 4) & 1;
  const char* tb = lds + K1_W + (8 * hh + q) * 192 + (16 * blk + 4 * p) * 2;
  const char* xb = lds + K1_X + wave * 12288 + r * 128;
  const int xsw = (r >> 1) & 7;
  const unsigned cfull = (unsigned)(size_t)(lds + K1_CTR), cdone = cfull + 32;
  bf16x8 xa[2], wa[2][3], xbf[2], wbf[2][3];
#define RD_HALF(XF, WF, slot_, k0_) do { const char* wp_ = tb + (slot_) * 12288; const char* xp_ = xb + (slot_) * 4096; \
    _Pragma("unroll") for (int ks = 0; ks < 2; ++ks) { \
      XF[ks] = *(const bf16x8*)(xp_ + (((2 * ((k0_) + ks) + hh) ^ xsw) * 16)); \
      _Pragma("unroll") for (int t = 0; t < 3; ++t) { const char* a_ = wp_ + ((k0_) + ks) * 16 * 192 + t * 64; \
        s16x4 lo_ = __builtin_amdgcn_ds_read_tr16_b64_v4i16((lds_s16x4_ptr)a_); \
        s16x4 hi_ = __builtin_amdgcn_ds_read_tr16_b64_v4i16((lds_s16x4_ptr)(a_ + 4 * 192)); \
        WF[ks][t] = __builtin_shufflevector(lo_, hi_, 0, 1, 2, 3, 4, 5, 6, 7); } } } while (0)
#define MM_HALF(XF, WF) do { _Pragma("unroll") for (int ks = 0; ks < 2; ++ks) _Pragma("unroll") for (int t = 0; t < 3; ++t) { \
      if (t >= 3 - NV) acc[t] = MFMA32(XF[ks], WF[ks][t], acc[t]); else acc[t] = MFMA32(WF[ks][t], XF[ks], acc[t]); } } while (0)
  int slot = 0;
  const unsigned one0 = lane == 0 ? 1u : 0u;
  lds_wait(cfull, 4);
  STAMP(1);
  RD_HALF(xa, wa, 0, 0);
#pragma nounroll
  for (int c = 0; c < 8; ++c) {
    RD_HALF(xbf, wbf, slot, 2);
    MM_HALF(xa, wa);
#pragma unroll
    for (int i = 0; i < 6; ++i) { __builtin_amdgcn_sched_group_barrier(0x8, 1, 0); __builtin_amdgcn_sched_group_barrier(0x100, 3, 0); }
    SBAR();
    unsigned pv;
    asm volatile("s_waitcnt lgkmcnt(0)\n\ts_mov_b64 exec, 1\n\tds_add_u32 %1, %2\n\ts_mov_b64 exec, -1\n\tds_read_b32 %0, %3" : "=&v"(pv) : "v"(cdone + 4 * c), "v"(one0), "v"(cfull + 4 * ((c + 1) & 7)) : "memory");
    slot = slot == 2 ? 0 : slot + 1;
    SBAR();
#pragma unroll
    for (int t = 0; t < 3; ++t) { if (t >= 3 - NV) acc[t] = MFMA32(xbf[0], wbf[0][t], acc[t]); else acc[t] = MFMA32(wbf[0][t], xbf[0], acc[t]); }
    SBAR();
    asm volatile("s_waitcnt lgkmcnt(0)" : "+v"(pv) :: "memory");
    if (c < 7) {
      if ((unsigned)__builtin_amdgcn_readfirstlane(pv) < 4u) lds_wait(cfull + 4 * (c + 1), 4);
      SBAR();
      RD_HALF(xa, wa, slot, 0);
    }
#pragma unroll
    for (int t = 0; t < 3; ++t) { if (t >= 3 - NV) acc[t] = MFMA32(xbf[1], wbf[1][t], acc[t]); else acc[t] = MFMA32(wbf[1][t], xbf[1], acc[t]); }
    SBAR();
  }
  STAMP(2);
  const int tt = tok0 >> 5;
#pragma unroll
  for (int t = 0; t < 3; ++t) {
    const int f0 = nb * 96 + t * 32;
    if (t >= 3 - NV) {
      const int h = ((f0 - 1024) >> 4) + (r >> 4), d = r & 15;
#pragma unroll
      for (int s = 0; s < 2; ++s) {
        u32x4 v;
#pragma unroll
        for (int j = 0; j < 4; ++j) v[j] = pack2(acc[t][8 * s + 2 * j], acc[t][8 * s + 2 * j + 1]);
        char* vb = ws + WS_V + (size_t)h * VHEAD + (2 * tt + s) * VSTEP + hh * 288;
        st16(vb + d * 16, v);
        if (d < 2) { const unsigned o1 = d ? 0u : 0x3f803f80u; u32x4 one = {o1, o1, o1, o1}; st16(vb + 256 + d * 16, one); }
      }
    } else {
      const bool isQ = f0 < 512;
      const float sc = isQ ? QSCALE : 1.f;
#pragma unroll
      for (int a = 0; a < 2; ++a) {
        const int h = ((f0 & 511) >> 4) + a;
        u32x4 v; float n2 = 0.f;
#pragma unroll
        for (int j = 0; j < 4; ++j) {
          const float e0 = acc[t][8 * a + 2 * j] * sc, e1 = acc[t][8 * a + 2 * j + 1] * sc;
          v[j] = pack2(e0, e1);
          n2 = __builtin_fmaf(e0, e0, __builtin_fmaf(e1, e1, n2));
        }
        st16(ws + (isQ ? WS_Q : WS_K) + ((size_t)(h * 64 + tt) * 64 + lane) * 16, v);
        n2 = add_other_half(n2);
        const float nmx = row16_max(n2);
        float* dst = isQ ? (float*)(ws + WS_QN) + h * 2048 + tok0 + r : (float*)(ws + WS_KM) + (h * 64 + tt) * 2 + (lane >> 4);
        if (isQ ? (hh == 0) : ((lane & 47) == 0)) st4(dst, F2U(isQ ? n2 : nmx));
      }
    }
  }
  STAMP(3);
}

struct cf { float r, i; };
DI cf cmul(cf a, cf b) { cf c; c.r = a.r * b.r - a.i * b.i; c.i = a.r * b.i + a.i * b.r; return c; }
__device__ __noinline__ float exp_precise(float x) { return (float)exp((double)x); }
DI float exp_bf16(float x) {
  float e = __builtin_expf(x);
  const int b = (int)(F2U(e) & 0xffffu) - 0x8000;
  if (__builtin_expect(b >= -4 && b <= 4, 0)) e = exp_precise(x);
  return bfr(e);
}
DI cf cexp_pos(float ar, float ai, float p) {
  const float e = __builtin_amdgcn_exp2f(ar * p * 1.4426950408889634f);
  const float y = ai * p;
  const float c_hi = 0.15915494309189535f, c_lo = (float)(0.15915494309189533577 - (double)0.15915494309189535f);
  const float fr = __builtin_fmaf(y, c_lo, __builtin_fmaf(y, c_hi, -__builtin_rintf(y * c_hi)));
  cf o; o.r = e * __builtin_amdgcn_cosf(fr); o.i = e * __builtin_amdgcn_sinf(fr);
  return o;
}
DI void aux_waves(int b, int g, int gw, const u16* u, const u16* logA, const u16* Aim, const u16* Cr, const u16* Ci, const u16* logdt,
                  const u16* wo, char* ws, char* lds) {
  const int lane = TID & 63, gt = gw * 64 + lane;
  const int d = 2 * b + g;
  unsigned* ctr = (unsigned*)(lds + K1_CTR) + 16 + g;
  unsigned nbar = 0;
#define GBAR() do { nbar += 4; lds_barrier(ctr, nbar); } while (0)
  char* area = lds + K1_AUX + g * KG_AREA;
  const u16 p_dt = logdt[d];
  const int po = d * NS + lane;
  const u16 pa0 = logA[po], pa1 = logA[po + 64], pi0 = Aim[po], pi1 = Aim[po + 64], pr0 = Cr[po], pr1 = Cr[po + 64], pc0 = Ci[po], pc1 = Ci[po + 64];
  STAMP(1);
  unsigned* EA = (unsigned*)area + lane;
  const float dt = exp_bf16(bf2f(p_dt));
  f32x4 acc[3];
#pragma unroll
  for (int m = 0; m < 3; ++m) { acc[m][0] = 0.f; acc[m][1] = 0.f; acc[m][2] = 0.f; acc[m][3] = 0.f; }
  const int i16 = lane & 15, kq = lane >> 4;
#define PUT(i_, v_) do { const unsigned hi_ = pack2((v_).r, (v_).i); EA[(i_) * (KG_ROW / 4)] = hi_; EA[(i_) * (KG_ROW / 4) + 68] = pack2((v_).r - bf_lo(hi_), (v_).i - bf_hi(hi_)); } while (0)
#pragma nounroll
  for (int h = 0; h < 2; ++h) {
    const float ar = -exp_bf16(bf2f(h ? pa1 : pa0)) * dt, ai = bf2f(h ? pi1 : pi0) * dt;
    cf C; C.r = 2.f * bf2f(h ? pr1 : pr0) * dt; C.i = -2.f * bf2f(h ? pc1 : pc0) * dt;
    if (gw == 0) {
      cf G[8]; G[1] = cexp_pos(ar, -ai, 1.f);
      const cf CH = cmul(C, cexp_pos(ar, -ai, 128.f));
      G[2] = cmul(G[1], G[1]); G[3] = cmul(G[2], G[1]); G[4] = cmul(G[2], G[2]); G[5] = cmul(G[4], G[1]); G[6] = cmul(G[4], G[2]); G[7] = cmul(G[4], G[3]);
      PUT(0, C); PUT(8, CH);
#pragma unroll
      for (int i = 1; i < 8; ++i) { cf v = cmul(C, G[i]); PUT(i, v); cf v2 = cmul(CH, G[i]); PUT(8 + i, v2); }
    } else if (gw == 1) {
      const cf z2 = cexp_pos(ar, -ai, 2.f), h1 = cexp_pos(ar, -ai, 128.f);
      cf c2 = cmul(C, cexp_pos(ar, -ai, 256.f)), c4 = cmul(C, cexp_pos(ar, -ai, 512.f));
      const cf z4 = cmul(z2, z2), z6 = cmul(z4, z2);
      cf c3 = cmul(c2, h1);
      PUT(16, c2); { cf v = cmul(c2, z2); PUT(17, v); v = cmul(c2, z4); PUT(18, v); v = cmul(c2, z6); PUT(19, v); }
      PUT(20, c3); { cf v = cmul(c3, z2); PUT(21, v); v = cmul(c3, z4); PUT(22, v); v = cmul(c3, z6); PUT(23, v); }
#pragma unroll
      for (int k = 0; k < 4; ++k) { PUT(24 + 2 * k, c4); cf v = cmul(c4, z4); PUT(25 + 2 * k, v); if (k < 3) c4 = cmul(c4, h1); }
    } else if (gw == 2) {
      const cf h1 = cexp_pos(ar, -ai, 128.f);
      cf c8 = cmul(C, cexp_pos(ar, -ai, 1024.f)), c12 = cmul(C, cexp_pos(ar, -ai, 1536.f));
#pragma unroll
      for (int k = 0; k < 5; ++k) { if (k < 4) { PUT(32 + k, c8); if (k < 3) c8 = cmul(c8, h1); } PUT(36 + k, c12); if (k < 4) c12 = cmul(c12, h1); }
    } else {
      const cf w1 = cexp_pos(ar, ai, 8.f);
      cf F = w1, F8 = cexp_pos(ar, ai, 64.f);
      { cf one; one.r = 1.f; one.i = 0.f; PUT(41, one); }
      PUT(49, F8);
#pragma unroll
      for (int j = 1; j < 8; ++j) { PUT(41 + j, F); F8 = cmul(F8, w1); PUT(49 + j, F8); if (j < 7) F = cmul(F, w1); }
    }
    if (h == 0) STAMP(5);
    GBAR();
    {
      const char* bp = area + (41 + i16) * KG_ROW + gw * 64 + kq * 16;
      const bf16x8 bh = *(const bf16x8*)bp, bl = *(const bf16x8*)(bp + 272);
#pragma unroll
      for (int m = 0; m < 3; ++m) {
        const char* apz = area + (i16 + 16 * m) * KG_ROW + gw * 64 + kq * 16;
        const bf16x8 ah = *(const bf16x8*)apz, al = *(const bf16x8*)(apz + 272);
        acc[m] = __builtin_amdgcn_mfma_f32_16x16x32_bf16(ah, bh, acc[m], 0, 0, 0);
        acc[m] = __builtin_amdgcn_mfma_f32_16x16x32_bf16(ah, bl, acc[m], 0, 0, 0);
        acc[m] = __builtin_amdgcn_mfma_f32_16x16x32_bf16(al, bh, acc[m], 0, 0, 0);
      }
    }
    GBAR();
    if (h == 0) STAMP(6);
  }
  STAMP(2);
  float* tab = (float*)area;
#pragma unroll
  for (int m = 0; m < 3; ++m)
#pragma unroll
    for (int j = 0; j < 4; ++j) tab[gw * 768 + (16 * m + 4 * kq + j) * 16 + i16] = acc[m][j];
  GBAR();
  STAMP(3);
#pragma unroll
  for (int i = 0; i < 3; ++i) {
    const int e = gt + 256 * i;
    const float k = (tab[e] + tab[768 + e]) + (tab[1536 + e] + tab[2304 + e]);
    st4((unsigned*)(ws + WS_KT) + d * 768 + e, F2U(k));
  }
  STAMP(4);
}

NOPK __global__ __launch_bounds__(1024, 4) void k_pre(const u16* __restrict__ u, const u16* __restrict__ logA, const u16* __restrict__ Aim,
                                                const u16* __restrict__ Cr, const u16* __restrict__ Ci, const u16* __restrict__ logdt,
                                                const u16* __restrict__ x, const u16* __restrict__ w, const u16* __restrict__ wo, char* __restrict__ ws) {
  extern __shared__ __attribute__((aligned(16))) char lds[];
  const int wave = __builtin_amdgcn_readfirstlane(TID >> 6), b = BID;
  STAMP(0);
  GSTAMP(0, 0, ws);
  if (TID < 32) ((unsigned*)(lds + K1_CTR))[TID] = 0u;
  SYNC();
  const int xcd = b & 7, idx = b >> 3;
  const int mb = (xcd & 3) * 4 + (idx & 3), nb = (xcd >> 2) * 8 + (idx >> 2);
  if (wave < 4) {
    gemm_loader(b, mb, nb, wave, x, w, u, wo, ws, lds);
    STAMP(1);
  } else if (wave < 8) {
    if (nb < 10) gemm_consumer<0>(mb, nb, wave - 4, ws, lds);
    else if (nb == 10) gemm_consumer<1>(mb, nb, wave - 4, ws, lds);
    else gemm_consumer<3>(mb, nb, wave - 4, ws, lds);
  } else {
    aux_waves(b, (wave - 8) >> 2, (wave - 8) & 3, u, logA, Aim, Cr, Ci, logdt, wo, ws, lds);
  }
  GSTAMP(0, 1, ws);
}

constexpr int LDS_CP = 2 * CHIMG;
constexpr int LDS_AP = LDS_CP + 4 * 2304 * 4;
constexpr int LDS_FL = LDS_AP + 4 * 2 * 9 * 64 * 4;
constexpr int LDS_K2 = LDS_FL + 64;

#define GLOAD(dst, voff, sbase, off) asm volatile("global_load_dwordx4 %0, %1, %2 offset:" #off : "=v"(dst) : "v"(voff), "s"(sbase))
DI void exp_pack(const f32x16& s, bf16x8& pa, bf16x8& pb) {
  u32x4 a, b;
#pragma unroll
  for (int j = 0; j < 4; ++j) a[j] = pack2(__builtin_amdgcn_exp2f(s[2 * j]), __builtin_amdgcn_exp2f(s[2 * j + 1]));
#pragma unroll
  for (int j = 0; j < 4; ++j) b[j] = pack2(__builtin_amdgcn_exp2f(s[8 + 2 * j]), __builtin_amdgcn_exp2f(s[8 + 2 * j + 1]));
  pa = __builtin_bit_cast(bf16x8, a); pb = __builtin_bit_cast(bf16x8, b);
}
#define EP2(s_, j_) pack2(__builtin_amdgcn_exp2f((s_)[2 * (j_)]), __builtin_amdgcn_exp2f((s_)[2 * (j_) + 1]))
DI void attn_step(const char*& kp, const char*& vp, unsigned ko, unsigned vo, bf16x8& kc, bf16x8& kn, bf16x8& vca, bf16x8& vcb, bf16x8& vna, bf16x8& vnb,
                  bf16x8 q0, bf16x8 q1, const f32x16& nm0, const f32x16& nm1, f32x16& s0, f32x16& o0, f32x16& o1) {
  u32x4 pa, pb;
  pa[0] = EP2(s0, 0); pa[1] = EP2(s0, 1);
  SBAR();
  f32x16 s1 = MFMA32(kc, q1, nm1);
  SBAR();
  GLOAD(vna, vo, vp, 1152); GLOAD(vnb, vo, vp, 1728);
  GLOAD(kc, ko, kp, 2048);
  kp += 1024; vp += 2 * VSTEP;
  SBAR();
  pa[2] = EP2(s0, 2); pa[3] = EP2(s0, 3);
  asm volatile("s_waitcnt vmcnt(3)" : "+v"(vca), "+v"(vcb), "+v"(kn));
  SBAR();
  o0 = MFMA32(vca, __builtin_bit_cast(bf16x8, pa), o0);
  SBAR();
  pb[0] = EP2(s0, 4); pb[1] = EP2(s0, 5); pb[2] = EP2(s0, 6); pb[3] = EP2(s0, 7);
  SBAR();
  o0 = MFMA32(vcb, __builtin_bit_cast(bf16x8, pb), o0);
  SBAR();
  pa[0] = EP2(s1, 0); pa[1] = EP2(s1, 1);
  SBAR();
  s0 = MFMA32(kn, q0, nm0);
  SBAR();
  pa[2] = EP2(s1, 2); pa[3] = EP2(s1, 3);
  SBAR();
  o1 = MFMA32(vca, __builtin_bit_cast(bf16x8, pa), o1);
  SBAR();
  pb[0] = EP2(s1, 4); pb[1] = EP2(s1, 5); pb[2] = EP2(s1, 6); pb[3] = EP2(s1, 7);
  SBAR();
  o1 = MFMA32(vcb, __builtin_bit_cast(bf16x8, pb), o1);
  SBAR();
}
#define ATTN_FIRST_LOADS() do { GLOAD(kA, ko, kp, 0); GLOAD(vAa, vo, vp, 0); GLOAD(vAb, vo, vp, 576); GLOAD(kB, ko, kp, 1024); } while (0)
DI void attn_main(const char* kp, const char* vp, unsigned ko, unsigned vo, bf16x8 q0, bf16x8 q1, const f32x16& nm0, const f32x16& nm1, f32x16& o0, f32x16& o1,
                  bf16x8& kA, bf16x8& kB, bf16x8& vAa, bf16x8& vAb) {
#pragma unroll
  for (int i = 0; i < 16; ++i) { o0[i] = 0.f; o1[i] = 0.f; }
  bf16x8 vBa, vBb;
  asm volatile("" : "+v"(q0), "+v"(q1));
  asm volatile("s_waitcnt vmcnt(3)" : "+v"(kA));
  f32x16 s0 = MFMA32(kA, q0, nm0);
  for (int it = 0; it < 16; ++it) {
    attn_step(kp, vp, ko, vo, kA, kB, vAa, vAb, vBa, vBb, q0, q1, nm0, nm1, s0, o0, o1);
    attn_step(kp, vp, ko, vo, kB, kA, vBa, vBb, vAa, vAb, q0, q1, nm0, nm1, s0, o0, o1);
  }
  asm volatile("s_waitcnt vmcnt(0)" : "+v"(kA), "+v"(kB), "+v"(vAa), "+v"(vAb), "+v"(vBa), "+v"(vBb));
}

NOPK __global__ __launch_bounds__(768, 3) void k_attn_conv(char* __restrict__ ws) {
  extern __shared__ __attribute__((aligned(16))) char lds[];
  const int tid = TID, wave = __builtin_amdgcn_readfirstlane(tid >> 6), lane = tid & 63, r = lane & 31, hh = lane >> 5;
  const int b = BID;
  const int h = (b & 7) * 4 + ((b >> 3) & 3), qt = b >> 5;
#define STAMP2(i)
  STAMP2(0);
  GSTAMP(1, 0, ws);
  const int qg = wave & 3, kh = (wave >> 2) & 1;
  bf16x8 q0, q1; f32x16 nm0, nm1, o0, o1;
  const char* kp = ws + WS_K + (size_t)(h * 64 + kh * 32) * 1024;
  const char* vp = ws + WS_V + (size_t)h * VHEAD + (size_t)(kh * 64) * VSTEP;
  const unsigned ko = lane * 16, vo = hh * 288 + (r < 17 ? r : 17) * 16;
  const int qtile = qt * 8 + qg * 2;
  const int cw = wave - 8, cch = cw >> 1, chalf = cw & 1;
  float cinv0 = 1.f, cinv1 = 1.f;
  bf16x8 kA, kB, vAa, vAb;
  f32x2 kmv; float n0, n1;
  if (wave < 8) {
    q0 = *(const bf16x8*)(ws + WS_Q + ((size_t)(h * 64 + qtile) * 64 + lane) * 16);
    q1 = *(const bf16x8*)(ws + WS_Q + ((size_t)(h * 64 + qtile + 1) * 64 + lane) * 16);
    kmv = ((const f32x2*)(ws + WS_KM))[h * 64 + lane];
    n0 = ((const float*)(ws + WS_QN))[h * 2048 + qtile * 32 + r]; n1 = ((const float*)(ws + WS_QN))[h * 2048 + qtile * 32 + 32 + r];
    SBAR();
    ATTN_FIRST_LOADS();
  }
  if (tid >= 512 && tid < 528) ((unsigned*)(lds + LDS_FL))[tid - 512] = 0u;
  asm volatile("s_waitcnt lgkmcnt(0)" ::: "memory");
  __builtin_amdgcn_s_barrier();
  asm volatile("" ::: "memory");
  if (wave < 8) {
    float km = row16_max(__builtin_fmaxf(kmv[0], kmv[1]));
    km = __builtin_fmaxf(__builtin_fmaxf(__builtin_bit_cast(float, __builtin_amdgcn_readlane(__builtin_bit_cast(int, km), 0)), __builtin_bit_cast(float, __builtin_amdgcn_readlane(__builtin_bit_cast(int, km), 16))),
               __builtin_fmaxf(__builtin_bit_cast(float, __builtin_amdgcn_readlane(__builtin_bit_cast(int, km), 32)), __builtin_bit_cast(float, __builtin_amdgcn_readlane(__builtin_bit_cast(int, km), 48))));
    const float m0 = __builtin_amdgcn_sqrtf(n0 * km) * 1.01f, m1 = __builtin_amdgcn_sqrtf(n1 * km) * 1.01f;
#pragma unroll
    for (int i = 0; i < 16; ++i) { nm0[i] = -m0; nm1[i] = -m1; }
  } else {
    __builtin_amdgcn_s_setprio(3);
    const int ct = chalf * 64 + lane;
    char* img = lds + cch * CHIMG;
    u16* tabl = (u16*)(lds + LDS_CP + cch * 2 * 2304 * 4);
    unsigned* mxs = (unsigned*)(lds + LDS_FL) + 12 + cch * 2;
    const char* ur = ws + WS_UR + (size_t)(2 * b + cch) * URROW;
    const u32x4* kt = (const u32x4*)(ws + WS_KT + (size_t)(2 * b + cch) * 3072);
    u32x4 tv0 = kt[ct], tv1 = {0, 0, 0, 0};
    if (ct < 36) tv1 = kt[128 + ct];
    u32x4 ua[3]; u32x2 ub[3]; u32x4 mp[2];
#pragma unroll
    for (int i = 0; i < 3; ++i) {
      const int g = ct + 128 * i;
      ua[i] = u32x4{0, 0, 0, 0}; ub[i] = u32x2{0, 0};
      if (g < 260) { ua[i] = *(const u32x4*)(ur + g * 16); ub[i] = *(const u32x2*)(ur + g * 16 + 16); }
    }
#pragma unroll
    for (int i = 0; i < 2; ++i) mp[i] = *(const u32x4*)(KMAP.v + (ct + 128 * i) * 8);
    { u32x4 z = {0, 0, 0, 0};
      *(u32x4*)(img + UIMG + ct * 16) = z; if (ct < 32) *(u32x4*)(img + UIMG + 2048 + ct * 16) = z; }
    unsigned mu = 0, mk = 0;
#pragma unroll
    for (int i = 0; i < 3; ++i)
#pragma unroll
      for (int j = 0; j < 4; ++j) { const unsigned a = ua[i][j] & 0x7fff7fffu; mu = UMAX(mu, UMAX(a << 16, a & 0xffff0000u)); }
#pragma unroll
    for (int j = 0; j < 4; ++j) mk = UMAX(mk, UMAX(tv0[j] & 0x7fffffffu, tv1[j] & 0x7fffffffu));
    mu = F2U(row16_max(U2F(mu & 0x7f800000u))); mk = F2U(row16_max(U2F(mk & 0x7f800000u)));
    if ((lane & 15) == 0) {
      asm volatile("ds_max_u32 %0, %1\n\tds_max_u32 %0, %2 offset:4" :: "v"((unsigned)(size_t)mxs), "v"(mu), "v"(mk) : "memory");
    }
    lds_barrier((unsigned*)(lds + LDS_FL) + 8 + cch, 2);
    const int eu = (int)(mxs[0] >> 23), ek = (int)(mxs[1] >> 23);
    const int su_e = 268 - eu > 253 ? 253 : 268 - eu, sk_e = 268 - ek > 253 ? 253 : 268 - ek;
    const float su = U2F((unsigned)su_e << 23), sk = U2F((unsigned)sk_e << 23);
    cinv0 = U2F((unsigned)(254 - su_e) << 23); cinv1 = U2F((unsigned)(254 - sk_e) << 23);
#define CVH(x_) __builtin_bit_cast(unsigned, __builtin_amdgcn_cvt_pkrtz(bf_lo(x_) * su, bf_hi(x_) * su))
#pragma unroll
    for (int i = 0; i < 3; ++i) {
      const int g = ct + 128 * i;
      if (g < 260) {
        const unsigned h0 = CVH(ua[i][0]), h1 = CVH(ua[i][1]), h2 = CVH(ua[i][2]), h3 = CVH(ua[i][3]), h4 = CVH(ub[i][0]), h5 = CVH(ub[i][1]);
        const unsigned a0 = __builtin_amdgcn_alignbit(h1, h0, 16), a1 = __builtin_amdgcn_alignbit(h2, h1, 16), a2 = __builtin_amdgcn_alignbit(h3, h2, 16),
                       a3 = __builtin_amdgcn_alignbit(h4, h3, 16), a4 = __builtin_amdgcn_alignbit(h5, h4, 16);
        const u32x4 c0 = {h0, h1, h2, h3}, c1 = {a0, a1, a2, a3}, c2 = {h1, h2, h3, h4}, c3 = {a1, a2, a3, a4};
        *(u32x4*)(img + g * 16) = c0; *(u32x4*)(img + UCOPY + g * 16) = c1;
        *(u32x4*)(img + 2 * UCOPY + g * 16) = c2; *(u32x4*)(img + 3 * UCOPY + g * 16) = c3;
      }
    }
    {
      typedef _Float16 h2_t __attribute__((ext_vector_type(2)));
      u32x2 t0, t1;
#pragma unroll
      for (int j = 0; j < 2; ++j) {
        h2_t a = {(_Float16)(U2F(tv0[2 * j]) * sk), (_Float16)(U2F(tv0[2 * j + 1]) * sk)}, c = {(_Float16)(U2F(tv1[2 * j]) * sk), (_Float16)(U2F(tv1[2 * j + 1]) * sk)};
        t0[j] = __builtin_bit_cast(unsigned, a); t1[j] = __builtin_bit_cast(unsigned, c);
      }
      *(u32x2*)(tabl + ct * 4) = t0;
      if (ct < 64) *(u32x2*)(tabl + 512 + ct * 4) = t1;
    }
    lds_barrier((unsigned*)(lds + LDS_FL) + 8 + cch, 4);
#pragma unroll
    for (int i = 0; i < 2; ++i) {
      const int g = ct + 128 * i;
      u32x4 vh;
#pragma unroll
      for (int j = 0; j < 4; ++j) vh[j] = (unsigned)tabl[mp[i][j] & 0xffff] | ((unsigned)tabl[mp[i][j] >> 16] << 16);
      *(u32x4*)(img + UIMG + (32 + (g >> 2)) * KROW + (g & 3) * 16) = vh;
    }
    lds_barrier((unsigned*)(lds + LDS_FL) + 8 + cch, 6);
    __builtin_amdgcn_s_setprio(0);
  }
  STAMP2(1);
  float* ap = (float*)(lds + LDS_AP);
  volatile int* fl = (volatile int*)(lds + LDS_FL);
  for (int pass = 0;; ++pass) {
    if (wave < 8) {
      attn_main(kp, vp, ko, vo, q0, q1, nm0, nm1, o0, o1, kA, kB, vAa, vAb);
      o0[8] = add_other_half(o0[8]); o1[8] = add_other_half(o1[8]);
      if (kh == 1) {
#pragma unroll
        for (int i = 0; i < 9; ++i) { ap[((qg * 2 + 0) * 9 + i) * 64 + lane] = o0[i]; ap[((qg * 2 + 1) * 9 + i) * 64 + lane] = o1[i]; }
      }
      const bool bad = !(o0[8] >= 8.2718061e-25f) || !(o1[8] >= 8.2718061e-25f);
      const int anybad = (__builtin_amdgcn_ballot_w64(bad) != 0);
      if (lane == 0) fl[wave] = anybad;
    } else if (pass == 0) {
      const int c = (3 - r) & 3;
      const char* ua = lds + cch * CHIMG + c * UCOPY + 2 * (2047 + 8 * hh - r - c);
      const char* kb = lds + cch * CHIMG + UIMG + (r + 32) * KROW + 16 * hh;
      f32x16 a0, a1;
#pragma unroll
      for (int i = 0; i < 16; ++i) { a0[i] = 0.f; a1[i] = 0.f; }
#pragma unroll 2
      for (int dd = 0; dd < 16; ++dd) {
        const int dl = chalf * 16 + dd;
#pragma unroll
        for (int s = 0; s < 2; ++s) {
          const char* up = ua - 64 * dl + 32 * s;
          const s16x4 x0 = *(const s16x4*)up, x1 = *(const s16x4*)(up + 8);
          const s16x4 y0 = *(const s16x4*)(up - 2048), y1 = *(const s16x4*)(up - 2048 + 8);
          const bf16x8 A0 = __builtin_shufflevector(x0, x1, 0, 1, 2, 3, 4, 5, 6, 7), A1 = __builtin_shufflevector(y0, y1, 0, 1, 2, 3, 4, 5, 6, 7);
          const char* kq = kb - KROW * dl + 32 * s;
          const bf16x8 B0 = *(const bf16x8*)kq, B1 = *(const bf16x8*)(kq + 32 * KROW);
          a0 = MFMA32H(A0, B0, a0); a1 = MFMA32H(A0, B1, a1);
          a1 = MFMA32H(A1, B0, a1);
        }
      }
      float* cp = (float*)(lds + LDS_CP) + cw * 2304;
#pragma unroll
      for (int g = 0; g < 4; ++g) {
        f32x4 v0 = {a0[4 * g], a0[4 * g + 1], a0[4 * g + 2], a0[4 * g + 3]}, v1 = {a1[4 * g], a1[4 * g + 1], a1[4 * g + 2], a1[4 * g + 3]};
        *(f32x4*)(cp + 36 * r + 8 * g + 4 * hh) = v0;
        *(f32x4*)(cp + 36 * (32 + r) + 8 * g + 4 * hh) = v1;
      }
      lds_barrier((unsigned*)(lds + LDS_FL) + 8 + cch, 8);
#pragma unroll
      for (int i = 0; i < 2; ++i) {
        const int g = chalf * 128 + i * 64 + lane;
        const float* c0 = (const float*)(lds + LDS_CP) + (2 * cch) * 2304 + 36 * (g >> 2) + 8 * (g & 3);
        const f32x4 a = (*(const f32x4*)c0 + *(const f32x4*)(c0 + 2304)) * cinv0 * cinv1, bq = (*(const f32x4*)(c0 + 4) + *(const f32x4*)(c0 + 2304 + 4)) * cinv0 * cinv1;
        u32x4 w = {pack2(a[0], a[1]), pack2(a[2], a[3]), pack2(bq[0], bq[1]), pack2(bq[2], bq[3])};
        st16(ws + WS_YT + ((size_t)(2 * b + cch) * L + 8 * g) * 2, w);
      }
    }
    STAMP2(2);
    SYNC();
    STAMP2(3);
    int redo = 0;
    if (pass == 0) {
#pragma unroll
      for (int i = 0; i < 8; ++i) redo |= fl[i];
    }
    if (!redo) break;
    if (wave < 8) {
      const char* kall = ws + WS_K + ((size_t)(h * 64) * 64 + lane) * 16;
      float m0 = -3.0e38f, m1 = -3.0e38f;
      f32x16 z;
#pragma unroll
      for (int i = 0; i < 16; ++i) z[i] = 0.f;
      for (int it = 0; it < 64; ++it) {
        const bf16x8 kf = *(const bf16x8*)(kall + it * 1024);
        const f32x16 s0 = MFMA32(kf, q0, z), s1 = MFMA32(kf, q1, z);
#pragma unroll
        for (int i = 0; i < 16; ++i) { m0 = __builtin_fmaxf(m0, s0[i]); m1 = __builtin_fmaxf(m1, s1[i]); }
      }
      m0 = max_other_half(m0); m1 = max_other_half(m1);
#pragma unroll
      for (int i = 0; i < 16; ++i) { nm0[i] = -m0; nm1[i] = -m1; }
      ATTN_FIRST_LOADS();
    }
    SYNC();
  }
  if (wave < 4) {
#pragma unroll
    for (int t = 0; t < 2; ++t) {
      float v[9];
#pragma unroll
      for (int i = 0; i < 9; ++i) v[i] = (t ? o1[i] : o0[i]) + ap[((qg * 2 + t) * 9 + i) * 64 + lane];
      const float inv = 1.f / v[8];
      u32x4 w;
#pragma unroll
      for (int j = 0; j < 4; ++j) w[j] = pack2(v[2 * j] * inv, v[2 * j + 1] * inv);
      st16(ws + WS_O + ((size_t)((qtile + t) * 32 + h) * 64 + lane) * 16, w);
    }
  }
  STAMP2(4);
  GSTAMP(1, 1, ws);
}

constexpr int RED_STRIDE = 68;
DI void out_role(int b, const char* ws, u16* out, char* lds) {
  const int tid = TID, wave = tid >> 6, lane = tid & 63, r = lane & 31, hh = lane >> 5;
  const int xcd = b & 7, idx = b >> 3, mt = xcd * 4 + (idx & 3), nt = idx >> 2;
  bf16x8 of[2][8], wf[2][8];
#pragma unroll
  for (int k = 0; k < 8; ++k) {
    const int h = wave * 8 + k;
#pragma unroll
    for (int i = 0; i < 2; ++i) {
      of[i][k] = *(const bf16x8*)(ws + WS_O + ((size_t)((2 * mt + i) * 32 + h) * 64 + lane) * 16);
      wf[i][k] = *(const bf16x8*)(ws + WS_WO + ((size_t)((2 * nt + i) * 32 + h) * 64 + lane) * 16);
    }
  }
  f32x16 acc[2][2];
#pragma unroll
  for (int i = 0; i < 2; ++i)
#pragma unroll
    for (int j = 0; j < 2; ++j)
#pragma unroll
      for (int e = 0; e < 16; ++e) acc[i][j][e] = 0.f;
#pragma unroll
  for (int k = 0; k < 8; ++k)
#pragma unroll
    for (int i = 0; i < 2; ++i)
#pragma unroll
      for (int j = 0; j < 2; ++j) acc[i][j] = MFMA32(of[i][k], wf[j][k], acc[i][j]);
  float* red = (float*)lds + wave * 64 * RED_STRIDE;
#pragma unroll
  for (int i = 0; i < 2; ++i)
#pragma unroll
    for (int j = 0; j < 2; ++j)
#pragma unroll
      for (int e = 0; e < 16; ++e) red[(32 * i + crow(e, hh)) * RED_STRIDE + 32 * j + r] = acc[i][j][e];
  SYNC();
  const int tok = tid >> 2, n0 = (tid & 3) * 16;
  const float* rp = (const float*)lds + tok * RED_STRIDE + n0;
  u32x4 w[2];
#pragma unroll
  for (int g = 0; g < 4; ++g) {
    f32x4 s = *(const f32x4*)(rp + 4 * g);
#pragma unroll
    for (int wv = 1; wv < 4; ++wv) s += *(const f32x4*)(rp + wv * 64 * RED_STRIDE + 4 * g);
    w[g >> 1][(g & 1) * 2] = pack2(s[0], s[1]); w[g >> 1][(g & 1) * 2 + 1] = pack2(s[2], s[3]);
  }
  u16* op = out + (size_t)(mt * 64 + tok) * 1024 + 512 + nt * 64 + n0;
  *(u32x4*)op = w[0]; *(u32x4*)(op + 8) = w[1];
}
DI void fin_role(int b, const char* ws, const u16* u, const u16* dsk, u16* out, char* lds) {
  const int tid = TID;
  const int l0 = (b >> 3) * 64, d0 = (b & 7) * 64;
  u16* t = (u16*)lds;
#pragma unroll
  for (int i = 0; i < 2; ++i) {
    const int idx = tid + 256 * i, row = idx >> 3, ch = idx & 7;
    *(u32x4*)(t + row * 72 + ch * 8) = *(const u32x4*)(ws + WS_YT + ((size_t)(d0 + row) * L + l0 + ch * 8) * 2);
  }
  SYNC();
#pragma unroll
  for (int i = 0; i < 2; ++i) {
    const int idx = tid + 256 * i, ll = idx >> 3, ch = idx & 7;
    const u32x4 uv = *(const u32x4*)(u + (size_t)(l0 + ll) * DM + d0 + ch * 8);
    const u32x4 dv = *(const u32x4*)(dsk + d0 + ch * 8);
    u32x4 v;
#pragma unroll
    for (int j = 0; j < 4; ++j) {
      const float y0 = bf2f(t[(ch * 8 + 2 * j) * 72 + ll]), y1 = bf2f(t[(ch * 8 + 2 * j + 1) * 72 + ll]);
      const unsigned sk = pack2(bf_lo(uv[j]) * bf_lo(dv[j]), bf_hi(uv[j]) * bf_hi(dv[j]));
      v[j] = pack2(y0 + bf_lo(sk), y1 + bf_hi(sk));
    }
    *(u32x4*)(out + (size_t)(l0 + ll) * 1024 + d0 + ch * 8) = v;
  }
}
NOPK __global__ __launch_bounds__(256) void k_out(const char* ws, const u16* u, const u16* dsk, u16* out) {
  extern __shared__ __attribute__((aligned(16))) char lds[];
  GSTAMP(2, 0, ws);
  if (BID < 256) out_role(BID, ws, out, lds);
  else fin_role(BID - 256, ws, u, dsk, out, lds);
  GSTAMP(2, 1, ws);
}

extern "C" void kernel_launch(void* const* d_in, const int* in_sizes, int n_in, void* d_out, int out_size, void* d_ws, size_t ws_size,
                              hipStream_t stream) {
  const u16* u = (const u16*)d_in[0]; const u16* logA = (const u16*)d_in[1]; const u16* Aim = (const u16*)d_in[2];
  const u16* Cr = (const u16*)d_in[3]; const u16* Ci = (const u16*)d_in[4]; const u16* logdt = (const u16*)d_in[5];
  const u16* dsk = (const u16*)d_in[6]; const u16* x = (const u16*)d_in[7]; const u16* wqkv = (const u16*)d_in[8]; const u16* wo = (const u16*)d_in[9];
  u16* out = (u16*)d_out; char* ws = (char*)d_ws;
  constexpr int LDS_OUT = 4 * 64 * RED_STRIDE * 4;
  (void)hipFuncSetAttribute((const void*)k_pre, hipFuncAttributeMaxDynamicSharedMemorySize, LDS_K1);
  (void)hipFuncSetAttribute((const void*)k_attn_conv, hipFuncAttributeMaxDynamicSharedMemorySize, LDS_K2);
  (void)hipFuncSetAttribute((const void*)k_out, hipFuncAttributeMaxDynamicSharedMemorySize, LDS_OUT);
  k_pre<<<256, 1024, LDS_K1, stream>>>(u, logA, Aim, Cr, Ci, logdt, x, wqkv, wo, ws);
  k_attn_conv<<<256, 768, LDS_K2, stream>>>(ws);
  k_out<<<512, 256, LDS_OUT, stream>>>(ws, u, dsk, out);
}
```

```cpp
#include <hip/hip_runtime.h>
#include <hip/hip_bf16.h>

#define TID ((int)__builtin_amdgcn_workitem_id_x())
#define BID ((int)__builtin_amdgcn_workgroup_id_x())
#define SYNC() do { __builtin_amdgcn_fence(__ATOMIC_RELEASE, "workgroup"); __builtin_amdgcn_s_barrier(); __builtin_amdgcn_fence(__ATOMIC_ACQUIRE, "workgroup"); } while (0)
#define UMAX(a, b) ((a) > (b) ? (a) : (b))
#define U2F(x) __builtin_bit_cast(float, (unsigned)(x))
#define F2U(x) __builtin_bit_cast(unsigned, (float)(x))
typedef unsigned short u16;
typedef short bf16x8 __attribute__((ext_vector_type(8)));
typedef short s16x4 __attribute__((ext_vector_type(4)));
typedef float f32x16 __attribute__((ext_vector_type(16)));
typedef float f32x4 __attribute__((ext_vector_type(4)));
typedef float f32x2 __attribute__((ext_vector_type(2)));
typedef unsigned u32x4 __attribute__((ext_vector_type(4)));
typedef unsigned u32x2 __attribute__((ext_vector_type(2)));
typedef __bf16 bf16x2_t __attribute__((ext_vector_type(2)));
typedef _Float16 f16x8 __attribute__((ext_vector_type(8)));
#define MFMA32H(a, b, c) __builtin_amdgcn_mfma_f32_32x32x16_f16(__builtin_bit_cast(f16x8, a), __builtin_bit_cast(f16x8, b), (c), 0, 0, 0)
typedef __attribute__((address_space(3))) s16x4* lds_s16x4_ptr;
#if defined(__HIP_DEVICE_COMPILE__)
#define NOPK __attribute__((target("no-packed-fp32-ops")))
#else
#define NOPK
#endif
#define DI NOPK __device__ __forceinline__
#define MFMA32(a, b, c) __builtin_amdgcn_mfma_f32_32x32x16_bf16((a), (b), (c), 0, 0, 0)
#define SBAR() __builtin_amdgcn_sched_barrier(0)
#define GLDS16(src, dst) __builtin_amdgcn_global_load_lds((const unsigned*)(src), (unsigned*)(dst), 16, 0, 0)

#define STAMP(i)

constexpr int L = 2048, DM = 512, NS = 128, NH = 32;
constexpr size_t MB = 1024 * 1024;
constexpr size_t WS_Q = 0, WS_K = 2 * MB, WS_V = 4 * MB, WS_QN = 6 * MB + 512 * 1024, WS_KM = 6 * MB + 768 * 1024,
                 WS_O = 7 * MB, WS_WO = 9 * MB, WS_YT = 10 * MB, WS_UR = 12 * MB, WS_KT = 15 * MB;
constexpr int URROW = 4224;
constexpr int VSTEP = 576;
constexpr int VHEAD = 128 * VSTEP;
constexpr float QSCALE = 0.25f * 1.4426950408889634f;
constexpr int UCOPY = 4160, UIMG = 4 * UCOPY, KROW = 80, KIMG = 96 * KROW, CHIMG = UIMG + KIMG;

#define GSTAMP(k, i, wsp)
DI unsigned pack2(float a, float b) { f32x2 v = {a, b}; bf16x2_t r = __builtin_convertvector(v, bf16x2_t); return __builtin_bit_cast(unsigned, r); }
DI void st16(void* p, u32x4 v) { asm volatile("global_store_dwordx4 %0, %1, off sc1\n\ts_nop 1" :: "v"(p), "v"(v) : "memory"); }
DI void st4(void* p, unsigned v) { asm volatile("global_store_dword %0, %1, off sc1" :: "v"(p), "v"(v) : "memory"); }
DI float bf_lo(unsigned p) { return U2F(p << 16); }
DI float bf_hi(unsigned p) { return U2F(p & 0xffff0000u); }
DI float bf2f(u16 v) { return U2F(((unsigned)v) << 16); }
DI float bfr(float x) { return bf_lo(pack2(x, 0.f)); }
DI int crow(int reg, int hh) { return (reg & 3) + 8 * (reg >> 2) + 4 * hh; }
template <int CTRL> DI float dppf(float x) { return __builtin_bit_cast(float, __builtin_amdgcn_update_dpp(0, __builtin_bit_cast(int, x), CTRL, 0xf, 0xf, false)); }
DI float add_other_half(float x) { auto rr = __builtin_amdgcn_permlane32_swap(F2U(x), F2U(x), false, false); return U2F(rr[0]) + U2F(rr[1]); }
DI float max_other_half(float x) { auto rr = __builtin_amdgcn_permlane32_swap(F2U(x), F2U(x), false, false); return __builtin_fmaxf(U2F(rr[0]), U2F(rr[1])); }
DI float row16_max(float x) { x = __builtin_fmaxf(x, dppf<0x128>(x)); x = __builtin_fmaxf(x, dppf<0x124>(x)); x = __builtin_fmaxf(x, dppf<0x122>(x)); x = __builtin_fmaxf(x, dppf<0x121>(x)); return x; }

DI void lds_barrier(unsigned* ctr, unsigned target) {
  const unsigned addr = (unsigned)(size_t)ctr;
  asm volatile("s_waitcnt lgkmcnt(0)" ::: "memory");
  if ((TID & 63) == 0) asm volatile("ds_add_u32 %0, %1" :: "v"(addr), "v"(1u) : "memory");
  for (;;) {
    unsigned v;
    asm volatile("ds_read_b32 %0, %1\n\ts_waitcnt lgkmcnt(0)" : "=v"(v) : "v"(addr) : "memory");
    if ((unsigned)__builtin_amdgcn_readfirstlane(v) >= target) break;
    __builtin_amdgcn_s_sleep(1);
  }
}

struct MapT { u16 v[2048]; };
constexpr MapT make_map() {
  MapT m{};
  for (int l = 0; l < 2048; ++l) {
    int P = l;
    if (l >= 256) {
      const int s = l >= 1024 ? 3 : (l >= 512 ? 2 : 1);
      int q = l >> s; const int rem = l & ((1 << s) - 1), half = 1 << (s - 1);
      if (rem > half || (rem == half && (q & 1))) ++q;
      P = q << s;
    }
    const int rem = P & 127, lo = rem >> 3, f = rem & 7, b = P >> 7;
    int i = 0;
    if (P < 256) i = 8 * b + f;
    else if (P < 512) i = 16 + 4 * (b - 2) + (f >> 1);
    else if (P < 1024) i = 24 + 2 * (b - 4) + (f >> 2);
    else i = 32 + (b - 8);
    m.v[l] = (u16)(i * 16 + lo);
  }
  return m;
}
__device__ const MapT KMAP = make_map();

constexpr int K1_W = 0, K1_X = 36864, K1_AUX = 86016, KG_ROW = 544, KG_AREA = 57 * KG_ROW, K1_UT = K1_AUX + 2 * KG_AREA, K1_CTR = K1_UT + 64 * 72 * 2, LDS_K1 = K1_CTR + 128;

DI void lds_signal(unsigned addr) {
  if ((TID & 63) == 0) asm volatile("ds_add_u32 %0, %1" :: "v"(addr), "v"(1u) : "memory");
}
DI void lds_wait(unsigned addr, unsigned target) {
  for (;;) {
    unsigned v;
    asm volatile("ds_read_b32 %0, %1\n\ts_waitcnt lgkmcnt(0)" : "=v"(v) : "v"(addr) : "memory");
    if ((unsigned)__builtin_amdgcn_readfirstlane(v) >= target) break;
    __builtin_amdgcn_s_sleep(1);
  }
}

DI void gemm_loader(int b, int mb, int nb, int wave, const u16* __restrict__ x, const u16* __restrict__ w, const u16* __restrict__ u,
                    const u16* __restrict__ wo, char* __restrict__ ws, char* lds) {
  const int lane = TID & 63, gt = TID;
  const int tok0 = mb * 128 + wave * 32;
  const u16* wsrc[3]; const u16* xsrc[4];
#pragma unroll
  for (int i = 0; i < 3; ++i) { const int idx = (wave * 3 + i) * 64 + lane, row = idx / 12, ch = idx - row * 12; wsrc[i] = w + (size_t)row * 1536 + nb * 96 + ch * 8; }
#pragma unroll
  for (int j = 0; j < 4; ++j) { const int idx = j * 64 + lane, row = idx >> 3, ch = (idx & 7) ^ ((row >> 1) & 7); xsrc[j] = x + (size_t)(tok0 + row) * 512 + ch * 8; }
  char* wdst = lds + K1_W + (wave * 3 * 64 + lane) * 16;
  char* xdst = lds + K1_X + wave * 12288 + lane * 16;
  const unsigned cfull = (unsigned)(size_t)(lds + K1_CTR), cdone = cfull + 32;
  const int l0 = (b >> 3) * 64, d0 = (b & 7) * 64;
  int slot = 0;
#pragma nounroll
  for (int c = 0; c < 8; ++c) {
    if (c >= 2) {
      asm volatile("s_waitcnt vmcnt(7)" ::: "memory");
      lds_signal(cfull + 4 * (c - 2));
      if (c >= 3) lds_wait(cdone + 4 * (c - 3), 4);
    }
#pragma unroll
    for (int i = 0; i < 3; ++i) { GLDS16(wsrc[i], wdst + slot * 12288 + i * 1024); wsrc[i] += 64 * 1536; }
#pragma unroll
    for (int j = 0; j < 4; ++j) { GLDS16(xsrc[j], xdst + slot * 4096 + j * 1024); xsrc[j] += 64; }
    slot = slot == 2 ? 0 : slot + 1;
  }
  char* ut = lds + K1_UT;
#pragma unroll
  for (int i = 0; i < 2; ++i) { const int idx = gt + 256 * i, row = idx >> 3, ch = (idx & 7) ^ ((row >> 1) & 7); GLDS16(u + (size_t)(l0 + row) * DM + d0 + ch * 8, ut + (wave * 64 + 256 * i) * 16 + lane * 16); }
  asm volatile("s_waitcnt vmcnt(9)" ::: "memory"); lds_signal(cfull + 24);
  asm volatile("s_waitcnt vmcnt(2)" ::: "memory"); lds_signal(cfull + 28);
  if (gt < 128) {
    const int gi = b * 128 + gt;
    const int ln = gi & 63, h = (gi >> 6) & 31, nt = gi >> 11, c = ln & 31, hh = ln >> 5;
    u32x4 v;
#pragma unroll
    for (int j = 0; j < 4; ++j) {
      const int dd = (j < 2 ? 4 * hh + 2 * j : 8 + 4 * hh + 2 * (j - 2));
      v[j] = (unsigned)wo[(size_t)(h * 16 + dd) * DM + nt * 32 + c] | ((unsigned)wo[(size_t)(h * 16 + dd + 1) * DM + nt * 32 + c] << 16);
    }
    st16(ws + WS_WO + (size_t)gi * 16, v);
  }
  asm volatile("s_waitcnt vmcnt(0)" ::: "memory");
  lds_barrier((unsigned*)(lds + K1_CTR) + 18, 4);
#pragma unroll
  for (int i = 0; i < 2; ++i) {
    const int idx = gt + 256 * i, dd = idx >> 3, gi = idx & 7;
    u32x4 v;
#pragma unroll
    for (int j = 0; j < 4; ++j) {
      const int r1 = 63 - 8 * gi - 2 * j, r0 = r1 - 1;
      const int co = (((dd >> 3) ^ ((r1 >> 1) & 7)) * 8 + (dd & 7)) * 2;
      v[j] = (unsigned)*(const u16*)(ut + r1 * 128 + co) | ((unsigned)*(const u16*)(ut + r0 * 128 + co) << 16);
    }
    st16(ws + WS_UR + (size_t)(d0 + dd) * URROW + (1984 - l0 + 8 * gi) * 2, v);
    if (l0 == 0) { u32x4 z = {0, 0, 0, 0}; st16(ws + WS_UR + (size_t)(d0 + dd) * URROW + 4096 + gi * 16, z); }
  }
}

template <int NV>
DI void gemm_consumer(int mb, int nb, int wave, char* __restrict__ ws, char* lds) {
  const int lane = TID & 63, r = lane & 31, hh = lane >> 5;
  const int tok0 = mb * 128 + wave * 32;
  f32x16 acc[3];
#pragma unroll
  for (int t = 0; t < 3; ++t)
#pragma unroll
    for (int i = 0; i < 16; ++i) acc[t][i] = 0.f;
  const int q = (lane & 15) >> 2, p = lane & 3, blk = (lane >> 4) & 1;
  const char* tb = lds + K1_W + (8 * hh + q) * 192 + (16 * blk + 4 * p) * 2;
  const char* xb = lds + K1_X + wave * 12288 + r * 128;
  const int xsw = (r >> 1) & 7;
  const unsigned cfull = (unsigned)(size_t)(lds + K1_CTR), cdone = cfull + 32;
  bf16x8 xa[2], wa[2][3], xbf[2], wbf[2][3];
#define RD_HALF(XF, WF, slot_, k0_) do { const char* wp_ = tb + (slot_) * 12288; const char* xp_ = xb + (slot_) * 4096; \
    _Pragma("unroll") for (int ks = 0; ks < 2; ++ks) { \
      XF[ks] = *(const bf16x8*)(xp_ + (((2 * ((k0_) + ks) + hh) ^ xsw) * 16)); \
      _Pragma("unroll") for (int t = 0; t < 3; ++t) { const char* a_ = wp_ + ((k0_) + ks) * 16 * 192 + t * 64; \
        s16x4 lo_ = __builtin_amdgcn_ds_read_tr16_b64_v4i16((lds_s16x4_ptr)a_); \
        s16x4 hi_ = __builtin_amdgcn_ds_read_tr16_b64_v4i16((lds_s16x4_ptr)(a_ + 4 * 192)); \
        WF[ks][t] = __builtin_shufflevector(lo_, hi_, 0, 1, 2, 3, 4, 5, 6, 7); } } } while (0)
#define MM_HALF(XF, WF) do { _Pragma("unroll") for (int ks = 0; ks < 2; ++ks) _Pragma("unroll") for (int t = 0; t < 3; ++t) { \
      if (t >= 3 - NV) acc[t] = MFMA32(XF[ks], WF[ks][t], acc[t]); else acc[t] = MFMA32(WF[ks][t], XF[ks], acc[t]); } } while (0)
  int slot = 0;
  const unsigned one0 = lane == 0 ? 1u : 0u;
  lds_wait(cfull, 4);
  STAMP(1);
  RD_HALF(xa, wa, 0, 0);
#pragma nounroll
  for (int c = 0; c < 8; ++c) {
    RD_HALF(xbf, wbf, slot, 2);
    MM_HALF(xa, wa);
#pragma unroll
    for (int i = 0; i < 6; ++i) { __builtin_amdgcn_sched_group_barrier(0x8, 1, 0); __builtin_amdgcn_sched_group_barrier(0x100, 3, 0); }
    SBAR();
    unsigned pv;
    asm volatile("s_waitcnt lgkmcnt(0)\n\ts_mov_b64 exec, 1\n\tds_add_u32 %1, %2\n\ts_mov_b64 exec, -1\n\tds_read_b32 %0, %3" : "=&v"(pv) : "v"(cdone + 4 * c), "v"(one0), "v"(cfull + 4 * ((c + 1) & 7)) : "memory");
    slot = slot == 2 ? 0 : slot + 1;
    SBAR();
#pragma unroll
    for (int t = 0; t < 3; ++t) { if (t >= 3 - NV) acc[t] = MFMA32(xbf[0], wbf[0][t], acc[t]); else acc[t] = MFMA32(wbf[0][t], xbf[0], acc[t]); }
    SBAR();
    asm volatile("s_waitcnt lgkmcnt(0)" : "+v"(pv) :: "memory");
    if (c < 7) {
      if ((unsigned)__builtin_amdgcn_readfirstlane(pv) < 4u) lds_wait(cfull + 4 * (c + 1), 4);
      SBAR();
      RD_HALF(xa, wa, slot, 0);
    }
#pragma unroll
    for (int t = 0; t < 3; ++t) { if (t >= 3 - NV) acc[t] = MFMA32(xbf[1], wbf[1][t], acc[t]); else acc[t] = MFMA32(wbf[1][t], xbf[1], acc[t]); }
    SBAR();
  }
  STAMP(2);
  const int tt = tok0 >> 5;
#pragma unroll
  for (int t = 0; t < 3; ++t) {
    const int f0 = nb * 96 + t * 32;
    if (t >= 3 - NV) {
      const int h = ((f0 - 1024) >> 4) + (r >> 4), d = r & 15;
#pragma unroll
      for (int s = 0; s < 2; ++s) {
        u32x4 v;
#pragma unroll
        for (int j = 0; j < 4; ++j) v[j] = pack2(acc[t][8 * s + 2 * j], acc[t][8 * s + 2 * j + 1]);
        char* vb = ws + WS_V + (size_t)h * VHEAD + (2 * tt + s) * VSTEP + hh * 288;
        st16(vb + d * 16, v);
        if (d < 2) { const unsigned o1 = d ? 0u : 0x3f803f80u; u32x4 one = {o1, o1, o1, o1}; st16(vb + 256 + d * 16, one); }
      }
    } else {
      const bool isQ = f0 < 512;
      const float sc = isQ ? QSCALE : 1.f;
#pragma unroll
      for (int a = 0; a < 2; ++a) {
        const int h = ((f0 & 511) >> 4) + a;
        u32x4 v; float n2 = 0.f;
#pragma unroll
        for (int j = 0; j < 4; ++j) {
          const float e0 = acc[t][8 * a + 2 * j] * sc, e1 = acc[t][8 * a + 2 * j + 1] * sc;
          v[j] = pack2(e0, e1);
          n2 = __builtin_fmaf(e0, e0, __builtin_fmaf(e1, e1, n2));
        }
        st16(ws + (isQ ? WS_Q : WS_K) + ((size_t)(h * 64 + tt) * 64 + lane) * 16, v);
        if (!isQ) {
          const float nmx = row16_max(add_other_half(n2));
          if ((lane & 47) == 0) st4((float*)(ws + WS_KM) + (h * 64 + tt) * 2 + (lane >> 4), F2U(nmx));
        }
      }
    }
  }
  STAMP(3);
}

struct cf { float r, i; };
DI cf cmul(cf a, cf b) { cf c; c.r = a.r * b.r - a.i * b.i; c.i = a.r * b.i + a.i * b.r; return c; }
__device__ __noinline__ float exp_precise(float x) { return (float)exp((double)x); }
DI float exp_bf16(float x) {
  float e = __builtin_expf(x);
  const int b = (int)(F2U(e) & 0xffffu) - 0x8000;
  if (__builtin_expect(b >= -4 && b <= 4, 0)) e = exp_precise(x);
  return bfr(e);
}
DI cf cexp_pos(float ar, float ai, float p) {
  const float e = __builtin_amdgcn_exp2f(ar * p * 1.4426950408889634f);
  const float y = ai * p;
  const float c_hi = 0.15915494309189535f, c_lo = (float)(0.15915494309189533577 - (double)0.15915494309189535f);
  const float fr = __builtin_fmaf(y, c_lo, __builtin_fmaf(y, c_hi, -__builtin_rintf(y * c_hi)));
  cf o; o.r = e * __builtin_amdgcn_cosf(fr); o.i = e * __builtin_amdgcn_sinf(fr);
  return o;
}
DI void aux_waves(int b, int g, int gw, const u16* u, const u16* logA, const u16* Aim, const u16* Cr, const u16* Ci, const u16* logdt,
                  const u16* wo, char* ws, char* lds) {
  const int lane = TID & 63, gt = gw * 64 + lane;
  const int d = 2 * b + g;
  unsigned* ctr = (unsigned*)(lds + K1_CTR) + 16 + g;
  unsigned nbar = 0;
#define GBAR() do { nbar += 4; lds_barrier(ctr, nbar); } while (0)
  char* area = lds + K1_AUX + g * KG_AREA;
  const u16 p_dt = logdt[d];
  const int po = d * NS + lane;
  const u16 pa0 = logA[po], pa1 = logA[po + 64], pi0 = Aim[po], pi1 = Aim[po + 64], pr0 = Cr[po], pr1 = Cr[po + 64], pc0 = Ci[po], pc1 = Ci[po + 64];
  STAMP(1);
  unsigned* EA = (unsigned*)area + lane;
  const float dt = exp_bf16(bf2f(p_dt));
  f32x4 acc[3];
#pragma unroll
  for (int m = 0; m < 3; ++m) { acc[m][0] = 0.f; acc[m][1] = 0.f; acc[m][2] = 0.f; acc[m][3] = 0.f; }
  const int i16 = lane & 15, kq = lane >> 4;
#define PUT(i_, v_) do { const unsigned hi_ = pack2((v_).r, (v_).i); EA[(i_) * (KG_ROW / 4)] = hi_; EA[(i_) * (KG_ROW / 4) + 68] = pack2((v_).r - bf_lo(hi_), (v_).i - bf_hi(hi_)); } while (0)
#pragma nounroll
  for (int h = 0; h < 2; ++h) {
    const float ar = -exp_bf16(bf2f(h ? pa1 : pa0)) * dt, ai = bf2f(h ? pi1 : pi0) * dt;
    cf C; C.r = 2.f * bf2f(h ? pr1 : pr0) * dt; C.i = -2.f * bf2f(h ? pc1 : pc0) * dt;
    if (gw == 0) {
      cf G[8]; G[1] = cexp_pos(ar, -ai, 1.f);
      const cf CH = cmul(C, cexp_pos(ar, -ai, 128.f));
      G[2] = cmul(G[1], G[1]); G[3] = cmul(G[2], G[1]); G[4] = cmul(G[2], G[2]); G[5] = cmul(G[4], G[1]); G[6] = cmul(G[4], G[2]); G[7] = cmul(G[4], G[3]);
      PUT(0, C); PUT(8, CH);
#pragma unroll
      for (int i = 1; i < 8; ++i) { cf v = cmul(C, G[i]); PUT(i, v); cf v2 = cmul(CH, G[i]); PUT(8 + i, v2); }
    } else if (gw == 1) {
      const cf z2 = cexp_pos(ar, -ai, 2.f), h1 = cexp_pos(ar, -ai, 128.f);
      cf c2 = cmul(C, cexp_pos(ar, -ai, 256.f)), c4 = cmul(C, cexp_pos(ar, -ai, 512.f));
      const cf z4 = cmul(z2, z2), z6 = cmul(z4, z2);
      cf c3 = cmul(c2, h1);
      PUT(16, c2); { cf v = cmul(c2, z2); PUT(17, v); v = cmul(c2, z4); PUT(18, v); v = cmul(c2, z6); PUT(19, v); }
      PUT(20, c3); { cf v = cmul(c3, z2); PUT(21, v); v = cmul(c3, z4); PUT(22, v); v = cmul(c3, z6); PUT(23, v); }
#pragma unroll
      for (int k = 0; k < 4; ++k) { PUT(24 + 2 * k, c4); cf v = cmul(c4, z4); PUT(25 + 2 * k, v); if (k < 3) c4 = cmul(c4, h1); }
    } else if (gw == 2) {
      const cf h1 = cexp_pos(ar, -ai, 128.f);
      cf c8 = cmul(C, cexp_pos(ar, -ai, 1024.f)), c12 = cmul(C, cexp_pos(ar, -ai, 1536.f));
#pragma unroll
      for (int k = 0; k < 5; ++k) { if (k < 4) { PUT(32 + k, c8); if (k < 3) c8 = cmul(c8, h1); } PUT(36 + k, c12); if (k < 4) c12 = cmul(c12, h1); }
    } else {
      const cf w1 = cexp_pos(ar, ai, 8.f);
      cf F = w1, F8 = cexp_pos(ar, ai, 64.f);
      { cf one; one.r = 1.f; one.i = 0.f; PUT(41, one); }
      PUT(49, F8);
#pragma unroll
      for (int j = 1; j < 8; ++j) { PUT(41 + j, F); F8 = cmul(F8, w1); PUT(49 + j, F8); if (j < 7) F = cmul(F, w1); }
    }
    if (h == 0) STAMP(5);
    GBAR();
    {
      const char* bp = area + (41 + i16) * KG_ROW + gw * 64 + kq * 16;
      const bf16x8 bh = *(const bf16x8*)bp, bl = *(const bf16x8*)(bp + 272);
#pragma unroll
      for (int m = 0; m < 3; ++m) {
        const char* apz = area + (i16 + 16 * m) * KG_ROW + gw * 64 + kq * 16;
        const bf16x8 ah = *(const bf16x8*)apz, al = *(const bf16x8*)(apz + 272);
        acc[m] = __builtin_amdgcn_mfma_f32_16x16x32_bf16(ah, bh, acc[m], 0, 0, 0);
        acc[m] = __builtin_amdgcn_mfma_f32_16x16x32_bf16(ah, bl, acc[m], 0, 0, 0);
        acc[m] = __builtin_amdgcn_mfma_f32_16x16x32_bf16(al, bh, acc[m], 0, 0, 0);
      }
    }
    GBAR();
    if (h == 0) STAMP(6);
  }
  STAMP(2);
  float* tab = (float*)area;
#pragma unroll
  for (int m = 0; m < 3; ++m)
#pragma unroll
    for (int j = 0; j < 4; ++j) tab[gw * 768 + (16 * m + 4 * kq + j) * 16 + i16] = acc[m][j];
  GBAR();
  STAMP(3);
  if (gt < 164) {
    const f32x4* t4 = (const f32x4*)tab + gt;
    const f32x4 k = (t4[0] + t4[192]) + (t4[384] + t4[576]);
    st16(ws + WS_KT + (size_t)d * 3072 + gt * 16, __builtin_bit_cast(u32x4, k));
  }
  STAMP(4);
}

NOPK __global__ __launch_bounds__(1024, 4) void k_pre(const u16* __restrict__ u, const u16* __restrict__ logA, const u16* __restrict__ Aim,
                                                const u16* __restrict__ Cr, const u16* __restrict__ Ci, const u16* __restrict__ logdt,
                                                const u16* __restrict__ x, const u16* __restrict__ w, const u16* __restrict__ wo, char* __restrict__ ws) {
  extern __shared__ __attribute__((aligned(16))) char lds[];
  const int wave = __builtin_amdgcn_readfirstlane(TID >> 6), b = BID;
  STAMP(0);
  GSTAMP(0, 0, ws);
  if (TID < 32) ((unsigned*)(lds + K1_CTR))[TID] = 0u;
  SYNC();
  const int xcd = b & 7, idx = b >> 3;
  const int mb = (xcd & 3) * 4 + (idx & 3), nb = (xcd >> 2) * 8 + (idx >> 2);
  if (wave < 4) {
    gemm_loader(b, mb, nb, wave, x, w, u, wo, ws, lds);
    STAMP(1);
  } else if (wave < 8) {
    if (nb < 10) gemm_consumer<0>(mb, nb, wave - 4, ws, lds);
    else if (nb == 10) gemm_consumer<1>(mb, nb, wave - 4, ws, lds);
    else gemm_consumer<3>(mb, nb, wave - 4, ws, lds);
  } else {
    aux_waves(b, (wave - 8) >> 2, (wave - 8) & 3, u, logA, Aim, Cr, Ci, logdt, wo, ws, lds);
  }
  GSTAMP(0, 1, ws);
}

constexpr int LDS_CP = 2 * CHIMG;
constexpr int LDS_AP = LDS_CP + 4 * 2304 * 4;
constexpr int LDS_FL = LDS_AP + 4 * 2 * 9 * 64 * 4;
constexpr int LDS_K2 = LDS_FL + 64;

#define GLOAD(dst, voff, sbase, off) asm volatile("global_load_dwordx4 %0, %1, %2 offset:" #off : "=v"(dst) : "v"(voff), "s"(sbase))
DI void exp_pack(const f32x16& s, bf16x8& pa, bf16x8& pb) {
  u32x4 a, b;
#pragma unroll
  for (int j = 0; j < 4; ++j) a[j] = pack2(__builtin_amdgcn_exp2f(s[2 * j]), __builtin_amdgcn_exp2f(s[2 * j + 1]));
#pragma unroll
  for (int j = 0; j < 4; ++j) b[j] = pack2(__builtin_amdgcn_exp2f(s[8 + 2 * j]), __builtin_amdgcn_exp2f(s[8 + 2 * j + 1]));
  pa = __builtin_bit_cast(bf16x8, a); pb = __builtin_bit_cast(bf16x8, b);
}
#define EP2(s_, j_) pack2(__builtin_amdgcn_exp2f((s_)[2 * (j_)]), __builtin_amdgcn_exp2f((s_)[2 * (j_) + 1]))
DI void attn_step(const char*& kp, const char*& vp, unsigned ko, unsigned vo, bf16x8& kc, bf16x8& kn, bf16x8& vca, bf16x8& vcb, bf16x8& vna, bf16x8& vnb,
                  bf16x8 q0, bf16x8 q1, const f32x16& nm0, const f32x16& nm1, f32x16& s0, f32x16& o0, f32x16& o1) {
  u32x4 pa, pb;
  pa[0] = EP2(s0, 0); pa[1] = EP2(s0, 1);
  SBAR();
  f32x16 s1 = MFMA32(kc, q1, nm1);
  SBAR();
  GLOAD(vna, vo, vp, 1152); GLOAD(vnb, vo, vp, 1728);
  GLOAD(kc, ko, kp, 2048);
  kp += 1024; vp += 2 * VSTEP;
  SBAR();
  pa[2] = EP2(s0, 2); pa[3] = EP2(s0, 3);
  asm volatile("s_waitcnt vmcnt(3)" : "+v"(vca), "+v"(vcb), "+v"(kn));
  SBAR();
  o0 = MFMA32(vca, __builtin_bit_cast(bf16x8, pa), o0);
  SBAR();
  pb[0] = EP2(s0, 4); pb[1] = EP2(s0, 5); pb[2] = EP2(s0, 6); pb[3] = EP2(s0, 7);
  SBAR();
  o0 = MFMA32(vcb, __builtin_bit_cast(bf16x8, pb), o0);
  SBAR();
  pa[0] = EP2(s1, 0); pa[1] = EP2(s1, 1);
  SBAR();
  s0 = MFMA32(kn, q0, nm0);
  SBAR();
  pa[2] = EP2(s1, 2); pa[3] = EP2(s1, 3);
  SBAR();
  o1 = MFMA32(vca, __builtin_bit_cast(bf16x8, pa), o1);
  SBAR();
  pb[0] = EP2(s1, 4); pb[1] = EP2(s1, 5); pb[2] = EP2(s1, 6); pb[3] = EP2(s1, 7);
  SBAR();
  o1 = MFMA32(vcb, __builtin_bit_cast(bf16x8, pb), o1);
  SBAR();
}
#define ATTN_FIRST_LOADS() do { GLOAD(kA, ko, kp, 0); GLOAD(vAa, vo, vp, 0); GLOAD(vAb, vo, vp, 576); GLOAD(kB, ko, kp, 1024); } while (0)
DI void attn_main(const char* kp, const char* vp, unsigned ko, unsigned vo, bf16x8 q0, bf16x8 q1, const f32x16& nm0, const f32x16& nm1, f32x16& o0, f32x16& o1,
                  bf16x8& kA, bf16x8& kB, bf16x8& vAa, bf16x8& vAb) {
#pragma unroll
  for (int i = 0; i < 16; ++i) { o0[i] = 0.f; o1[i] = 0.f; }
  bf16x8 vBa, vBb;
  asm volatile("" : "+v"(q0), "+v"(q1));
  asm volatile("s_waitcnt vmcnt(3)" : "+v"(kA));
  f32x16 s0 = MFMA32(kA, q0, nm0);
  for (int it = 0; it < 16; ++it) {
    attn_step(kp, vp, ko, vo, kA, kB, vAa, vAb, vBa, vBb, q0, q1, nm0, nm1, s0, o0, o1);
    attn_step(kp, vp, ko, vo, kB, kA, vBa, vBb, vAa, vAb, q0, q1, nm0, nm1, s0, o0, o1);
  }
  asm volatile("s_waitcnt vmcnt(0)" : "+v"(kA), "+v"(kB), "+v"(vAa), "+v"(vAb), "+v"(vBa), "+v"(vBb));
}

NOPK __global__ __launch_bounds__(768, 3) void k_attn_conv(char* __restrict__ ws) {
  extern __shared__ __attribute__((aligned(16))) char lds[];
  const int tid = TID, wave = __builtin_amdgcn_readfirstlane(tid >> 6), lane = tid & 63, r = lane & 31, hh = lane >> 5;
  const int b = BID;
  const int h = (b & 7) * 4 + ((b >> 3) & 3), qt = b >> 5;
#define STAMP2(i)
  STAMP2(0);
  GSTAMP(1, 0, ws);
  const int qg = wave & 3, kh = (wave >> 2) & 1;
  bf16x8 q0, q1; f32x16 nm0, nm1, o0, o1;
  const char* kp = ws + WS_K + (size_t)(h * 64 + kh * 32) * 1024;
  const char* vp = ws + WS_V + (size_t)h * VHEAD + (size_t)(kh * 64) * VSTEP;
  const unsigned ko = lane * 16, vo = hh * 288 + (r < 17 ? r : 17) * 16;
  const int qtile = qt * 8 + qg * 2;
  const int cw = wave - 8, cch = cw >> 1, chalf = cw & 1;
  float cinv0 = 1.f, cinv1 = 1.f;
  bf16x8 kA, kB, vAa, vAb;
  f32x2 kmv; float n0, n1;
  if (wave < 8) {
    q0 = *(const bf16x8*)(ws + WS_Q + ((size_t)(h * 64 + qtile) * 64 + lane) * 16);
    q1 = *(const bf16x8*)(ws + WS_Q + ((size_t)(h * 64 + qtile + 1) * 64 + lane) * 16);
    kmv = ((const f32x2*)(ws + WS_KM))[h * 64 + lane];
    SBAR();
    ATTN_FIRST_LOADS();
  }
  if (tid >= 512 && tid < 528) ((unsigned*)(lds + LDS_FL))[tid - 512] = 0u;
  asm volatile("s_waitcnt lgkmcnt(0)" ::: "memory");
  __builtin_amdgcn_s_barrier();
  asm volatile("" ::: "memory");
  if (wave < 8) {
    n0 = 0.f; n1 = 0.f;
#pragma unroll
    for (int j = 0; j < 4; ++j) {
      const unsigned a = __builtin_bit_cast(u32x4, q0)[j], c = __builtin_bit_cast(u32x4, q1)[j];
      n0 = __builtin_fmaf(bf_lo(a), bf_lo(a), __builtin_fmaf(bf_hi(a), bf_hi(a), n0));
      n1 = __builtin_fmaf(bf_lo(c), bf_lo(c), __builtin_fmaf(bf_hi(c), bf_hi(c), n1));
    }
    n0 = add_other_half(n0); n1 = add_other_half(n1);
    float km = row16_max(__builtin_fmaxf(kmv[0], kmv[1]));
    km = __builtin_fmaxf(__builtin_fmaxf(__builtin_bit_cast(float, __builtin_amdgcn_readlane(__builtin_bit_cast(int, km), 0)), __builtin_bit_cast(float, __builtin_amdgcn_readlane(__builtin_bit_cast(int, km), 16))),
               __builtin_fmaxf(__builtin_bit_cast(float, __builtin_amdgcn_readlane(__builtin_bit_cast(int, km), 32)), __builtin_bit_cast(float, __builtin_amdgcn_readlane(__builtin_bit_cast(int, km), 48))));
    const float m0 = __builtin_amdgcn_sqrtf(n0 * km) * 1.01f, m1 = __builtin_amdgcn_sqrtf(n1 * km) * 1.01f;
#pragma unroll
    for (int i = 0; i < 16; ++i) { nm0[i] = -m0; nm1[i] = -m1; }
  } else {
    __builtin_amdgcn_s_setprio(3);
    const int ct = chalf * 64 + lane;
    char* img = lds + cch * CHIMG;
    u16* tabl = (u16*)(lds + LDS_CP + cch * 2 * 2304 * 4);
    unsigned* mxs = (unsigned*)(lds + LDS_FL) + 12 + cch * 2;
    const char* ur = ws + WS_UR + (size_t)(2 * b + cch) * URROW;
    const u32x4* kt = (const u32x4*)(ws + WS_KT + (size_t)(2 * b + cch) * 3072);
    u32x4 tv0 = kt[ct], tv1 = {0, 0, 0, 0};
    if (ct < 36) tv1 = kt[128 + ct];
    u32x4 ua[3]; u32x2 ub[3]; u32x4 mp[2];
#pragma unroll
    for (int i = 0; i < 3; ++i) {
      const int g = ct + 128 * i;
      ua[i] = u32x4{0, 0, 0, 0}; ub[i] = u32x2{0, 0};
      if (g < 260) { ua[i] = *(const u32x4*)(ur + g * 16); ub[i] = *(const u32x2*)(ur + g * 16 + 16); }
    }
#pragma unroll
    for (int i = 0; i < 2; ++i) mp[i] = *(const u32x4*)(KMAP.v + (ct + 128 * i) * 8);
    { u32x4 z = {0, 0, 0, 0};
      *(u32x4*)(img + UIMG + ct * 16) = z; if (ct < 32) *(u32x4*)(img + UIMG + 2048 + ct * 16) = z; }
    unsigned mu = 0, mk = 0;
#pragma unroll
    for (int i = 0; i < 3; ++i)
#pragma unroll
      for (int j = 0; j < 4; ++j) { const unsigned a = ua[i][j] & 0x7fff7fffu; mu = UMAX(mu, UMAX(a << 16, a & 0xffff0000u)); }
#pragma unroll
    for (int j = 0; j < 4; ++j) mk = UMAX(mk, UMAX(tv0[j] & 0x7fffffffu, tv1[j] & 0x7fffffffu));
    mu = F2U(row16_max(U2F(mu & 0x7f800000u))); mk = F2U(row16_max(U2F(mk & 0x7f800000u)));
    if ((lane & 15) == 0) {
      asm volatile("ds_max_u32 %0, %1\n\tds_max_u32 %0, %2 offset:4" :: "v"((unsigned)(size_t)mxs), "v"(mu), "v"(mk) : "memory");
    }
    lds_barrier((unsigned*)(lds + LDS_FL) + 8 + cch, 2);
    const int eu = (int)(mxs[0] >> 23), ek = (int)(mxs[1] >> 23);
    const int su_e = 268 - eu > 253 ? 253 : 268 - eu, sk_e = 268 - ek > 253 ? 253 : 268 - ek;
    const float su = U2F((unsigned)su_e << 23), sk = U2F((unsigned)sk_e << 23);
    cinv0 = U2F((unsigned)(254 - su_e) << 23); cinv1 = U2F((unsigned)(254 - sk_e) << 23);
#define CVH(x_) __builtin_bit_cast(unsigned, __builtin_amdgcn_cvt_pkrtz(bf_lo(x_) * su, bf_hi(x_) * su))
#pragma unroll
    for (int i = 0; i < 3; ++i) {
      const int g = ct + 128 * i;
      if (g < 260) {
        const unsigned h0 = CVH(ua[i][0]), h1 = CVH(ua[i][1]), h2 = CVH(ua[i][2]), h3 = CVH(ua[i][3]), h4 = CVH(ub[i][0]), h5 = CVH(ub[i][1]);
        const unsigned a0 = __builtin_amdgcn_alignbit(h1, h0, 16), a1 = __builtin_amdgcn_alignbit(h2, h1, 16), a2 = __builtin_amdgcn_alignbit(h3, h2, 16),
                       a3 = __builtin_amdgcn_alignbit(h4, h3, 16), a4 = __builtin_amdgcn_alignbit(h5, h4, 16);
        const u32x4 c0 = {h0, h1, h2, h3}, c1 = {a0, a1, a2, a3}, c2 = {h1, h2, h3, h4}, c3 = {a1, a2, a3, a4};
        *(u32x4*)(img + g * 16) = c0; *(u32x4*)(img + UCOPY + g * 16) = c1;
        *(u32x4*)(img + 2 * UCOPY + g * 16) = c2; *(u32x4*)(img + 3 * UCOPY + g * 16) = c3;
      }
    }
    {
      typedef _Float16 h2_t __attribute__((ext_vector_type(2)));
      u32x2 t0, t1;
#pragma unroll
      for (int j = 0; j < 2; ++j) {
        h2_t a = {(_Float16)(U2F(tv0[2 * j]) * sk), (_Float16)(U2F(tv0[2 * j + 1]) * sk)}, c = {(_Float16)(U2F(tv1[2 * j]) * sk), (_Float16)(U2F(tv1[2 * j + 1]) * sk)};
        t0[j] = __builtin_bit_cast(unsigned, a); t1[j] = __builtin_bit_cast(unsigned, c);
      }
      *(u32x2*)(tabl + ct * 4) = t0;
      if (ct < 64) *(u32x2*)(tabl + 512 + ct * 4) = t1;
    }
    lds_barrier((unsigned*)(lds + LDS_FL) + 8 + cch, 4);
#pragma unroll
    for (int i = 0; i < 2; ++i) {
      const int g = ct + 128 * i;
      u32x4 vh;
#pragma unroll
      for (int j = 0; j < 4; ++j) vh[j] = (unsigned)tabl[mp[i][j] & 0xffff] | ((unsigned)tabl[mp[i][j] >> 16] << 16);
      *(u32x4*)(img + UIMG + (32 + (g >> 2)) * KROW + (g & 3) * 16) = vh;
    }
    lds_barrier((unsigned*)(lds + LDS_FL) + 8 + cch, 6);
    __builtin_amdgcn_s_setprio(0);
  }
  STAMP2(1);
  float* ap = (float*)(lds + LDS_AP);
  volatile int* fl = (volatile int*)(lds + LDS_FL);
  for (int pass = 0;; ++pass) {
    if (wave < 8) {
      attn_main(kp, vp, ko, vo, q0, q1, nm0, nm1, o0, o1, kA, kB, vAa, vAb);
      o0[8] = add_other_half(o0[8]); o1[8] = add_other_half(o1[8]);
      if (kh == 1) {
#pragma unroll
        for (int i = 0; i < 9; ++i) { ap[((qg * 2 + 0) * 9 + i) * 64 + lane] = o0[i]; ap[((qg * 2 + 1) * 9 + i) * 64 + lane] = o1[i]; }
      }
      const bool bad = !(o0[8] >= 8.2718061e-25f) || !(o1[8] >= 8.2718061e-25f);
      const int anybad = (__builtin_amdgcn_ballot_w64(bad) != 0);
      if (lane == 0) fl[wave] = anybad;
    } else if (pass == 0) {
      const int c = (3 - r) & 3;
      const char* ua = lds + cch * CHIMG + c * UCOPY + 2 * (2047 + 8 * hh - r - c);
      const char* kb = lds + cch * CHIMG + UIMG + (r + 32) * KROW + 16 * hh;
      f32x16 a0, a1;
#pragma unroll
      for (int i = 0; i < 16; ++i) { a0[i] = 0.f; a1[i] = 0.f; }
#pragma unroll 2
      for (int dd = 0; dd < 16; ++dd) {
        const int dl = chalf * 16 + dd;
#pragma unroll
        for (int s = 0; s < 2; ++s) {
          const char* up = ua - 64 * dl + 32 * s;
          const s16x4 x0 = *(const s16x4*)up, x1 = *(const s16x4*)(up + 8);
          const s16x4 y0 = *(const s16x4*)(up - 2048), y1 = *(const s16x4*)(up - 2048 + 8);
          const bf16x8 A0 = __builtin_shufflevector(x0, x1, 0, 1, 2, 3, 4, 5, 6, 7), A1 = __builtin_shufflevector(y0, y1, 0, 1, 2, 3, 4, 5, 6, 7);
          const char* kq = kb - KROW * dl + 32 * s;
          const bf16x8 B0 = *(const bf16x8*)kq, B1 = *(const bf16x8*)(kq + 32 * KROW);
          a0 = MFMA32H(A0, B0, a0); a1 = MFMA32H(A0, B1, a1);
          a1 = MFMA32H(A1, B0, a1);
        }
      }
      float* cp = (float*)(lds + LDS_CP) + cw * 2304;
#pragma unroll
      for (int g = 0; g < 4; ++g) {
        f32x4 v0 = {a0[4 * g], a0[4 * g + 1], a0[4 * g + 2], a0[4 * g + 3]}, v1 = {a1[4 * g], a1[4 * g + 1], a1[4 * g + 2], a1[4 * g + 3]};
        *(f32x4*)(cp + 36 * r + 8 * g + 4 * hh) = v0;
        *(f32x4*)(cp + 36 * (32 + r) + 8 * g + 4 * hh) = v1;
      }
      lds_barrier((unsigned*)(lds + LDS_FL) + 8 + cch, 8);
#pragma unroll
      for (int i = 0; i < 2; ++i) {
        const int g = chalf * 128 + i * 64 + lane;
        const float* c0 = (const float*)(lds + LDS_CP) + (2 * cch) * 2304 + 36 * (g >> 2) + 8 * (g & 3);
        const f32x4 a = (*(const f32x4*)c0 + *(const f32x4*)(c0 + 2304)) * cinv0 * cinv1, bq = (*(const f32x4*)(c0 + 4) + *(const f32x4*)(c0 + 2304 + 4)) * cinv0 * cinv1;
        u32x4 w = {pack2(a[0], a[1]), pack2(a[2], a[3]), pack2(bq[0], bq[1]), pack2(bq[2], bq[3])};
        st16(ws + WS_YT + ((size_t)(2 * b + cch) * L + 8 * g) * 2, w);
      }
    }
    STAMP2(2);
    SYNC();
    STAMP2(3);
    int redo = 0;
    if (pass == 0) {
#pragma unroll
      for (int i = 0; i < 8; ++i) redo |= fl[i];
    }
    if (!redo) break;
    if (wave < 8) {
      const char* kall = ws + WS_K + ((size_t)(h * 64) * 64 + lane) * 16;
      float m0 = -3.0e38f, m1 = -3.0e38f;
      f32x16 z;
#pragma unroll
      for (int i = 0; i < 16; ++i) z[i] = 0.f;
      for (int it = 0; it < 64; ++it) {
        const bf16x8 kf = *(const bf16x8*)(kall + it * 1024);
        const f32x16 s0 = MFMA32(kf, q0, z), s1 = MFMA32(kf, q1, z);
#pragma unroll
        for (int i = 0; i < 16; ++i) { m0 = __builtin_fmaxf(m0, s0[i]); m1 = __builtin_fmaxf(m1, s1[i]); }
      }
      m0 = max_other_half(m0); m1 = max_other_half(m1);
#pragma unroll
      for (int i = 0; i < 16; ++i) { nm0[i] = -m0; nm1[i] = -m1; }
      ATTN_FIRST_LOADS();
    }
    SYNC();
  }
  if (wave < 4) {
#pragma unroll
    for (int t = 0; t < 2; ++t) {
      float v[9];
#pragma unroll
      for (int i = 0; i < 9; ++i) v[i] = (t ? o1[i] : o0[i]) + ap[((qg * 2 + t) * 9 + i) * 64 + lane];
      const float inv = 1.f / v[8];
      u32x4 w;
#pragma unroll
      for (int j = 0; j < 4; ++j) w[j] = pack2(v[2 * j] * inv, v[2 * j + 1] * inv);
      st16(ws + WS_O + ((size_t)((qtile + t) * 32 + h) * 64 + lane) * 16, w);
    }
  }
  STAMP2(4);
  GSTAMP(1, 1, ws);
}

constexpr int RED_STRIDE = 68;
DI void out_role(int b, const char* ws, u16* out, char* lds) {
  const int tid = TID, wave = tid >> 6, lane = tid & 63, r = lane & 31, hh = lane >> 5;
  const int xcd = b & 7, idx = b >> 3, mt = xcd * 4 + (idx & 3), nt = idx >> 2;
  bf16x8 of[2][8], wf[2][8];
#pragma unroll
  for (int k = 0; k < 8; ++k) {
    const int h = wave * 8 + k;
#pragma unroll
    for (int i = 0; i < 2; ++i) {
      of[i][k] = *(const bf16x8*)(ws + WS_O + ((size_t)((2 * mt + i) * 32 + h) * 64 + lane) * 16);
      wf[i][k] = *(const bf16x8*)(ws + WS_WO + ((size_t)((2 * nt + i) * 32 + h) * 64 + lane) * 16);
    }
  }
  f32x16 acc[2][2];
#pragma unroll
  for (int i = 0; i < 2; ++i)
#pragma unroll
    for (int j = 0; j < 2; ++j)
#pragma unroll
      for (int e = 0; e < 16; ++e) acc[i][j][e] = 0.f;
#pragma unroll
  for (int k = 0; k < 8; ++k)
#pragma unroll
    for (int i = 0; i < 2; ++i)
#pragma unroll
      for (int j = 0; j < 2; ++j) acc[i][j] = MFMA32(of[i][k], wf[j][k], acc[i][j]);
  float* red = (float*)lds + wave * 64 * RED_STRIDE;
#pragma unroll
  for (int i = 0; i < 2; ++i)
#pragma unroll
    for (int j = 0; j < 2; ++j)
#pragma unroll
      for (int e = 0; e < 16; ++e) red[(32 * i + crow(e, hh)) * RED_STRIDE + 32 * j + r] = acc[i][j][e];
  SYNC();
  const int tok = tid >> 2, n0 = (tid & 3) * 16;
  const float* rp = (const float*)lds + tok * RED_STRIDE + n0;
  u32x4 w[2];
#pragma unroll
  for (int g = 0; g < 4; ++g) {
    f32x4 s = *(const f32x4*)(rp + 4 * g);
#pragma unroll
    for (int wv = 1; wv < 4; ++wv) s += *(const f32x4*)(rp + wv * 64 * RED_STRIDE + 4 * g);
    w[g >> 1][(g & 1) * 2] = pack2(s[0], s[1]); w[g >> 1][(g & 1) * 2 + 1] = pack2(s[2], s[3]);
  }
  u16* op = out + (size_t)(mt * 64 + tok) * 1024 + 512 + nt * 64 + n0;
  *(u32x4*)op = w[0]; *(u32x4*)(op + 8) = w[1];
}
DI void fin_role(int b, const char* ws, const u16* u, const u16* dsk, u16* out, char* lds) {
  const int tid = TID;
  const int l0 = (b >> 3) * 64, d0 = (b & 7) * 64;
  u16* t = (u16*)lds;
#pragma unroll
  for (int i = 0; i < 2; ++i) {
    const int idx = tid + 256 * i, row = idx >> 3, ch = idx & 7;
    *(u32x4*)(t + row * 72 + ch * 8) = *(const u32x4*)(ws + WS_YT + ((size_t)(d0 + row) * L + l0 + ch * 8) * 2);
  }
  SYNC();
#pragma unroll
  for (int i = 0; i < 2; ++i) {
    const int idx = tid + 256 * i, ll = idx >> 3, ch = idx & 7;
    const u32x4 uv = *(const u32x4*)(u + (size_t)(l0 + ll) * DM + d0 + ch * 8);
    const u32x4 dv = *(const u32x4*)(dsk + d0 + ch * 8);
    u32x4 v;
#pragma unroll
    for (int j = 0; j < 4; ++j) {
      const float y0 = bf2f(t[(ch * 8 + 2 * j) * 72 + ll]), y1 = bf2f(t[(ch * 8 + 2 * j + 1) * 72 + ll]);
      const unsigned sk = pack2(bf_lo(uv[j]) * bf_lo(dv[j]), bf_hi(uv[j]) * bf_hi(dv[j]));
      v[j] = pack2(y0 + bf_lo(sk), y1 + bf_hi(sk));
    }
    *(u32x4*)(out + (size_t)(l0 + ll) * 1024 + d0 + ch * 8) = v;
  }
}
NOPK __global__ __launch_bounds__(256) void k_out(const char* ws, const u16* u, const u16* dsk, u16* out) {
  extern __shared__ __attribute__((aligned(16))) char lds[];
  GSTAMP(2, 0, ws);
  if (BID < 256) out_role(BID, ws, out, lds);
  else fin_role(BID - 256, ws, u, dsk, out, lds);
  GSTAMP(2, 1, ws);
}

extern "C" void kernel_launch(void* const* d_in, const int* in_sizes, int n_in, void* d_out, int out_size, void* d_ws, size_t ws_size,
                              hipStream_t stream) {
  const u16* u = (const u16*)d_in[0]; const u16* logA = (const u16*)d_in[1]; const u16* Aim = (const u16*)d_in[2];
  const u16* Cr = (const u16*)d_in[3]; const u16* Ci = (const u16*)d_in[4]; const u16* logdt = (const u16*)d_in[5];
  const u16* dsk = (const u16*)d_in[6]; const u16* x = (const u16*)d_in[7]; const u16* wqkv = (const u16*)d_in[8]; const u16* wo = (const u16*)d_in[9];
  u16* out = (u16*)d_out; char* ws = (char*)d_ws;
  constexpr int LDS_OUT = 4 * 64 * RED_STRIDE * 4;
  (void)hipFuncSetAttribute((const void*)k_pre, hipFuncAttributeMaxDynamicSharedMemorySize, LDS_K1);
  (void)hipFuncSetAttribute((const void*)k_attn_conv, hipFuncAttributeMaxDynamicSharedMemorySize, LDS_K2);
  (void)hipFuncSetAttribute((const void*)k_out, hipFuncAttributeMaxDynamicSharedMemorySize, LDS_OUT);
  k_pre<<<256, 1024, LDS_K1, stream>>>(u, logA, Aim, Cr, Ci, logdt, x, wqkv, wo, ws);
  k_attn_conv<<<256, 768, LDS_K2, stream>>>(ws);
  k_out<<<512, 256, LDS_OUT, stream>>>(ws, u, dsk, out);
}
```

```cpp
#include <hip/hip_runtime.h>
#include <hip/hip_bf16.h>

#define TID ((int)__builtin_amdgcn_workitem_id_x())
#define BID ((int)__builtin_amdgcn_workgroup_id_x())
#define SYNC() do { __builtin_amdgcn_fence(__ATOMIC_RELEASE, "workgroup"); __builtin_amdgcn_s_barrier(); __builtin_amdgcn_fence(__ATOMIC_ACQUIRE, "workgroup"); } while (0)
#define UMAX(a, b) ((a) > (b) ? (a) : (b))
#define U2F(x) __builtin_bit_cast(float, (unsigned)(x))
#define F2U(x) __builtin_bit_cast(unsigned, (float)(x))
typedef unsigned short u16;
typedef short bf16x8 __attribute__((ext_vector_type(8)));
typedef short s16x4 __attribute__((ext_vector_type(4)));
typedef float f32x16 __attribute__((ext_vector_type(16)));
typedef float f32x4 __attribute__((ext_vector_type(4)));
typedef float f32x2 __attribute__((ext_vector_type(2)));
typedef unsigned u32x4 __attribute__((ext_vector_type(4)));
typedef unsigned u32x2 __attribute__((ext_vector_type(2)));
typedef __bf16 bf16x2_t __attribute__((ext_vector_type(2)));
typedef _Float16 f16x8 __attribute__((ext_vector_type(8)));
#define MFMA32H(a, b, c) __builtin_amdgcn_mfma_f32_32x32x16_f16(__builtin_bit_cast(f16x8, a), __builtin_bit_cast(f16x8, b), (c), 0, 0, 0)
typedef __attribute__((address_space(3))) s16x4* lds_s16x4_ptr;
#if defined(__HIP_DEVICE_COMPILE__)
#define NOPK __attribute__((target("no-packed-fp32-ops")))
#else
#define NOPK
#endif
#define DI NOPK __device__ __forceinline__
#define MFMA32(a, b, c) __builtin_amdgcn_mfma_f32_32x32x16_bf16((a), (b), (c), 0, 0, 0)
#define SBAR() __builtin_amdgcn_sched_barrier(0)
#define GLDS16(src, dst) __builtin_amdgcn_global_load_lds((const unsigned*)(src), (unsigned*)(dst), 16, 0, 0)

#define STAMP(i)

constexpr int L = 2048, DM = 512, NS = 128, NH = 32;
constexpr size_t MB = 1024 * 1024;
constexpr size_t WS_Q = 0, WS_K = 2 * MB, WS_V = 4 * MB, WS_QN = 6 * MB + 512 * 1024, WS_KM = 6 * MB + 768 * 1024,
                 WS_O = 7 * MB, WS_WO = 9 * MB, WS_YT = 10 * MB, WS_UR = 12 * MB, WS_KT = 15 * MB;
constexpr int URROW = 4224;
constexpr int VSTEP = 576;
constexpr int VHEAD = 128 * VSTEP;
constexpr float QSCALE = 0.25f * 1.4426950408889634f;
constexpr int UCOPY = 4160, UIMG = 4 * UCOPY, KROW = 80, KIMG = 96 * KROW, CHIMG = UIMG + KIMG;

#define GSTAMP(k, i, wsp)
DI unsigned pack2(float a, float b) { f32x2 v = {a, b}; bf16x2_t r = __builtin_convertvector(v, bf16x2_t); return __builtin_bit_cast(unsigned, r); }
DI void st16(void* p, u32x4 v) { asm volatile("global_store_dwordx4 %0, %1, off sc1\n\ts_nop 1" :: "v"(p), "v"(v) : "memory"); }
DI void st4(void* p, unsigned v) { asm volatile("global_store_dword %0, %1, off sc1" :: "v"(p), "v"(v) : "memory"); }
DI float bf_lo(unsigned p) { return U2F(p << 16); }
DI float bf_hi(unsigned p) { return U2F(p & 0xffff0000u); }
DI float bf2f(u16 v) { return U2F(((unsigned)v) << 16); }
DI float bfr(float x) { return bf_lo(pack2(x, 0.f)); }
DI int crow(int reg, int hh) { return (reg & 3) + 8 * (reg >> 2) + 4 * hh; }
template <int CTRL> DI float dppf(float x) { return __builtin_bit_cast(float, __builtin_amdgcn_update_dpp(0, __builtin_bit_cast(int, x), CTRL, 0xf, 0xf, false)); }
DI float add_other_half(float x) { auto rr = __builtin_amdgcn_permlane32_swap(F2U(x), F2U(x), false, false); return U2F(rr[0]) + U2F(rr[1]); }
DI float max_other_half(float x) { auto rr = __builtin_amdgcn_permlane32_swap(F2U(x), F2U(x), false, false); return __builtin_fmaxf(U2F(rr[0]), U2F(rr[1])); }
DI float row16_max(float x) { x = __builtin_fmaxf(x, dppf<0x128>(x)); x = __builtin_fmaxf(x, dppf<0x124>(x)); x = __builtin_fmaxf(x, dppf<0x122>(x)); x = __builtin_fmaxf(x, dppf<0x121>(x)); return x; }

DI void lds_barrier(unsigned* ctr, unsigned target) {
  const unsigned addr = (unsigned)(size_t)ctr;
  asm volatile("s_waitcnt lgkmcnt(0)" ::: "memory");
  if ((TID & 63) == 0) asm volatile("ds_add_u32 %0, %1" :: "v"(addr), "v"(1u) : "memory");
  for (;;) {
    unsigned v;
    asm volatile("ds_read_b32 %0, %1\n\ts_waitcnt lgkmcnt(0)" : "=v"(v) : "v"(addr) : "memory");
    if ((unsigned)__builtin_amdgcn_readfirstlane(v) >= target) break;
    __builtin_amdgcn_s_sleep(1);
  }
}

struct MapT { u16 v[2048]; };
constexpr MapT make_map() {
  MapT m{};
  for (int l = 0; l < 2048; ++l) {
    int P = l;
    if (l >= 256) {
      const int s = l >= 1024 ? 3 : (l >= 512 ? 2 : 1);
      int q = l >> s; const int rem = l & ((1 << s) - 1), half = 1 << (s - 1);
      if (rem > half || (rem == half && (q & 1))) ++q;
      P = q << s;
    }
    const int rem = P & 127, lo = rem >> 3, f = rem & 7, b = P >> 7;
    int i = 0;
    if (P < 256) i = 8 * b + f;
    else if (P < 512) i = 16 + 4 * (b - 2) + (f >> 1);
    else if (P < 1024) i = 24 + 2 * (b - 4) + (f >> 2);
    else i = 32 + (b - 8);
    m.v[l] = (u16)(i * 16 + lo);
  }
  return m;
}
__device__ const MapT KMAP = make_map();

constexpr int K1_W = 0, K1_X = 36864, K1_AUX = 86016, KG_ROW = 544, KG_AREA = 57 * KG_ROW, K1_UT = K1_AUX + 2 * KG_AREA, K1_CTR = K1_UT + 64 * 72 * 2, LDS_K1 = K1_CTR + 128;

DI void lds_signal(unsigned addr) {
  if ((TID & 63) == 0) asm volatile("ds_add_u32 %0, %1" :: "v"(addr), "v"(1u) : "memory");
}
DI void lds_wait(unsigned addr, unsigned target) {
  for (;;) {
    unsigned v;
    asm volatile("ds_read_b32 %0, %1\n\ts_waitcnt lgkmcnt(0)" : "=v"(v) : "v"(addr) : "memory");
    if ((unsigned)__builtin_amdgcn_readfirstlane(v) >= target) break;
    __builtin_amdgcn_s_sleep(1);
  }
}

DI void gemm_loader(int b, int mb, int nb, int wave, const u16* __restrict__ x, const u16* __restrict__ w, const u16* __restrict__ u,
                    const u16* __restrict__ wo, char* __restrict__ ws, char* lds) {
  const int lane = TID & 63, gt = TID;
  const int tok0 = mb * 128 + wave * 32;
  const u16* wsrc[3]; const u16* xsrc[4];
#pragma unroll
  for (int i = 0; i < 3; ++i) { const int idx = (wave * 3 + i) * 64 + lane, row = idx / 12, ch = idx - row * 12; wsrc[i] = w + (size_t)row * 1536 + nb * 96 + ch * 8; }
#pragma unroll
  for (int j = 0; j < 4; ++j) { const int idx = j * 64 + lane, row = idx >> 3, ch = (idx & 7) ^ ((row >> 1) & 7); xsrc[j] = x + (size_t)(tok0 + row) * 512 + ch * 8; }
  char* wdst = lds + K1_W + (wave * 3 * 64 + lane) * 16;
  char* xdst = lds + K1_X + wave * 12288 + lane * 16;
  const unsigned cfull = (unsigned)(size_t)(lds + K1_CTR), cdone = cfull + 32;
  const int l0 = (b >> 3) * 64, d0 = (b & 7) * 64;
  int slot = 0;
#pragma nounroll
  for (int c = 0; c < 8; ++c) {
    if (c >= 2) {
      asm volatile("s_waitcnt vmcnt(7)" ::: "memory");
      lds_signal(cfull + 4 * (c - 2));
      if (c >= 3) lds_wait(cdone + 4 * (c - 3), 4);
    }
#pragma unroll
    for (int i = 0; i < 3; ++i) { GLDS16(wsrc[i], wdst + slot * 12288 + i * 1024); wsrc[i] += 64 * 1536; }
#pragma unroll
    for (int j = 0; j < 4; ++j) { GLDS16(xsrc[j], xdst + slot * 4096 + j * 1024); xsrc[j] += 64; }
    slot = slot == 2 ? 0 : slot + 1;
  }
  asm volatile("s_waitcnt vmcnt(7)" ::: "memory"); lds_signal(cfull + 24);
  asm volatile("s_waitcnt vmcnt(0)" ::: "memory"); lds_signal(cfull + 28);
}

template <int NV>
DI void gemm_consumer(int mb, int nb, int wave, char* __restrict__ ws, char* lds) {
  const int lane = TID & 63, r = lane & 31, hh = lane >> 5;
  const int tok0 = mb * 128 + wave * 32;
  f32x16 acc[3];
#pragma unroll
  for (int t = 0; t < 3; ++t)
#pragma unroll
    for (int i = 0; i < 16; ++i) acc[t][i] = 0.f;
  const int q = (lane & 15) >> 2, p = lane & 3, blk = (lane >> 4) & 1;
  const char* tb = lds + K1_W + (8 * hh + q) * 192 + (16 * blk + 4 * p) * 2;
  const char* xb = lds + K1_X + wave * 12288 + r * 128;
  const int xsw = (r >> 1) & 7;
  const unsigned cfull = (unsigned)(size_t)(lds + K1_CTR), cdone = cfull + 32;
  bf16x8 xa[2], wa[2][3], xbf[2], wbf[2][3];
#define RD_HALF(XF, WF, slot_, k0_) do { const char* wp_ = tb + (slot_) * 12288; const char* xp_ = xb + (slot_) * 4096; \
    _Pragma("unroll") for (int ks = 0; ks < 2; ++ks) { \
      XF[ks] = *(const bf16x8*)(xp_ + (((2 * ((k0_) + ks) + hh) ^ xsw) * 16)); \
      _Pragma("unroll") for (int t = 0; t < 3; ++t) { const char* a_ = wp_ + ((k0_) + ks) * 16 * 192 + t * 64; \
        s16x4 lo_ = __builtin_amdgcn_ds_read_tr16_b64_v4i16((lds_s16x4_ptr)a_); \
        s16x4 hi_ = __builtin_amdgcn_ds_read_tr16_b64_v4i16((lds_s16x4_ptr)(a_ + 4 * 192)); \
        WF[ks][t] = __builtin_shufflevector(lo_, hi_, 0, 1, 2, 3, 4, 5, 6, 7); } } } while (0)
#define MM_HALF(XF, WF) do { _Pragma("unroll") for (int ks = 0; ks < 2; ++ks) _Pragma("unroll") for (int t = 0; t < 3; ++t) { \
      if (t >= 3 - NV) acc[t] = MFMA32(XF[ks], WF[ks][t], acc[t]); else acc[t] = MFMA32(WF[ks][t], XF[ks], acc[t]); } } while (0)
  int slot = 0;
  const unsigned one0 = lane == 0 ? 1u : 0u;
  lds_wait(cfull, 4);
  STAMP(1);
  RD_HALF(xa, wa, 0, 0);
#pragma nounroll
  for (int c = 0; c < 8; ++c) {
    RD_HALF(xbf, wbf, slot, 2);
    MM_HALF(xa, wa);
#pragma unroll
    for (int i = 0; i < 6; ++i) { __builtin_amdgcn_sched_group_barrier(0x8, 1, 0); __builtin_amdgcn_sched_group_barrier(0x100, 3, 0); }
    SBAR();
    unsigned pv;
    asm volatile("s_waitcnt lgkmcnt(0)\n\ts_mov_b64 exec, 1\n\tds_add_u32 %1, %2\n\ts_mov_b64 exec, -1\n\tds_read_b32 %0, %3" : "=&v"(pv) : "v"(cdone + 4 * c), "v"(one0), "v"(cfull + 4 * ((c + 1) & 7)) : "memory");
    slot = slot == 2 ? 0 : slot + 1;
    SBAR();
#pragma unroll
    for (int t = 0; t < 3; ++t) { if (t >= 3 - NV) acc[t] = MFMA32(xbf[0], wbf[0][t], acc[t]); else acc[t] = MFMA32(wbf[0][t], xbf[0], acc[t]); }
    SBAR();
    asm volatile("s_waitcnt lgkmcnt(0)" : "+v"(pv) :: "memory");
    if (c < 7) {
      if ((unsigned)__builtin_amdgcn_readfirstlane(pv) < 4u) lds_wait(cfull + 4 * (c + 1), 4);
      SBAR();
      RD_HALF(xa, wa, slot, 0);
    }
#pragma unroll
    for (int t = 0; t < 3; ++t) { if (t >= 3 - NV) acc[t] = MFMA32(xbf[1], wbf[1][t], acc[t]); else acc[t] = MFMA32(wbf[1][t], xbf[1], acc[t]); }
    SBAR();
  }
  STAMP(2);
  const int tt = tok0 >> 5;
#pragma unroll
  for (int t = 0; t < 3; ++t) {
    const int f0 = nb * 96 + t * 32;
    if (t >= 3 - NV) {
      const int h = ((f0 - 1024) >> 4) + (r >> 4), d = r & 15;
#pragma unroll
      for (int s = 0; s < 2; ++s) {
        u32x4 v;
#pragma unroll
        for (int j = 0; j < 4; ++j) v[j] = pack2(acc[t][8 * s + 2 * j], acc[t][8 * s + 2 * j + 1]);
        char* vb = ws + WS_V + (size_t)h * VHEAD + (2 * tt + s) * VSTEP + hh * 288;
        st16(vb + d * 16, v);
        if (d < 2) { const unsigned o1 = d ? 0u : 0x3f803f80u; u32x4 one = {o1, o1, o1, o1}; st16(vb + 256 + d * 16, one); }
      }
    } else {
      const bool isQ = f0 < 512;
      const float sc = isQ ? QSCALE : 1.f;
#pragma unroll
      for (int a = 0; a < 2; ++a) {
        const int h = ((f0 & 511) >> 4) + a;
        u32x4 v; float n2 = 0.f;
#pragma unroll
        for (int j = 0; j < 4; ++j) {
          const float e0 = acc[t][8 * a + 2 * j] * sc, e1 = acc[t][8 * a + 2 * j + 1] * sc;
          v[j] = pack2(e0, e1);
          n2 = __builtin_fmaf(e0, e0, __builtin_fmaf(e1, e1, n2));
        }
        st16(ws + (isQ ? WS_Q : WS_K) + ((size_t)(h * 64 + tt) * 64 + lane) * 16, v);
        if (!isQ) {
          const float nmx = row16_max(add_other_half(n2));
          if ((lane & 47) == 0) st4((float*)(ws + WS_KM) + (h * 64 + tt) * 2 + (lane >> 4), F2U(nmx));
        }
      }
    }
  }
  STAMP(3);
}

struct cf { float r, i; };
DI cf cmul(cf a, cf b) { cf c; c.r = a.r * b.r - a.i * b.i; c.i = a.r * b.i + a.i * b.r; return c; }
__device__ __noinline__ float exp_precise(float x) { return (float)exp((double)x); }
DI float exp_bf16(float x) {
  float e = __builtin_expf(x);
  const int b = (int)(F2U(e) & 0xffffu) - 0x8000;
  if (__builtin_expect(b >= -4 && b <= 4, 0)) e = exp_precise(x);
  return bfr(e);
}
DI cf cexp_pos(float ar, float ai, float p) {
  const float e = __builtin_amdgcn_exp2f(ar * p * 1.4426950408889634f);
  const float y = ai * p;
  const float c_hi = 0.15915494309189535f, c_lo = (float)(0.15915494309189533577 - (double)0.15915494309189535f);
  const float fr = __builtin_fmaf(y, c_lo, __builtin_fmaf(y, c_hi, -__builtin_rintf(y * c_hi)));
  cf o; o.r = e * __builtin_amdgcn_cosf(fr); o.i = e * __builtin_amdgcn_sinf(fr);
  return o;
}
DI void aux_waves(int b, int g, int gw, const u16* u, const u16* logA, const u16* Aim, const u16* Cr, const u16* Ci, const u16* logdt,
                  const u16* wo, char* ws, char* lds) {
  const int lane = TID & 63, gt = gw * 64 + lane;
  const int d = 2 * b + g;
  unsigned* ctr = (unsigned*)(lds + K1_CTR) + 16 + g;
  unsigned nbar = 0;
#define GBAR() do { nbar += 4; lds_barrier(ctr, nbar); } while (0)
  char* area = lds + K1_AUX + g * KG_AREA;
  const u16 p_dt = logdt[d];
  const int po = d * NS + lane;
  const u16 pa0 = logA[po], pa1 = logA[po + 64], pi0 = Aim[po], pi1 = Aim[po + 64], pr0 = Cr[po], pr1 = Cr[po + 64], pc0 = Ci[po], pc1 = Ci[po + 64];
  const int l0 = (b >> 3) * 64, d0 = (b & 7) * 64;
  char* ut = lds + K1_UT;
  if (g == 0) {
#pragma unroll
    for (int i = 0; i < 2; ++i) { const int idx = gt + 256 * i, row = idx >> 3, ch = (idx & 7) ^ ((row >> 1) & 7); GLDS16(u + (size_t)(l0 + row) * DM + d0 + ch * 8, ut + idx * 16); }
  } else if (gw >= 2) {
    const int gi = b * 128 + gt - 128;
    const int ln = gi & 63, h = (gi >> 6) & 31, nt = gi >> 11, c = ln & 31, hh = ln >> 5;
    u32x4 v;
#pragma unroll
    for (int j = 0; j < 4; ++j) {
      const int dd = (j < 2 ? 4 * hh + 2 * j : 8 + 4 * hh + 2 * (j - 2));
      v[j] = (unsigned)wo[(size_t)(h * 16 + dd) * DM + nt * 32 + c] | ((unsigned)wo[(size_t)(h * 16 + dd + 1) * DM + nt * 32 + c] << 16);
    }
    st16(ws + WS_WO + (size_t)gi * 16, v);
  }
  STAMP(1);
  unsigned* EA = (unsigned*)area + lane;
  const float dt = exp_bf16(bf2f(p_dt));
  f32x4 acc[3];
#pragma unroll
  for (int m = 0; m < 3; ++m) { acc[m][0] = 0.f; acc[m][1] = 0.f; acc[m][2] = 0.f; acc[m][3] = 0.f; }
  const int i16 = lane & 15, kq = lane >> 4;
#define PUT(i_, v_) do { const unsigned hi_ = pack2((v_).r, (v_).i); EA[(i_) * (KG_ROW / 4)] = hi_; EA[(i_) * (KG_ROW / 4) + 68] = pack2((v_).r - bf_lo(hi_), (v_).i - bf_hi(hi_)); } while (0)
#pragma nounroll
  for (int h = 0; h < 2; ++h) {
    const float ar = -exp_bf16(bf2f(h ? pa1 : pa0)) * dt, ai = bf2f(h ? pi1 : pi0) * dt;
    cf C; C.r = 2.f * bf2f(h ? pr1 : pr0) * dt; C.i = -2.f * bf2f(h ? pc1 : pc0) * dt;
    if (gw == 0) {
      cf G[8]; G[1] = cexp_pos(ar, -ai, 1.f);
      const cf CH = cmul(C, cexp_pos(ar, -ai, 128.f));
      G[2] = cmul(G[1], G[1]); G[3] = cmul(G[2], G[1]); G[4] = cmul(G[2], G[2]); G[5] = cmul(G[4], G[1]); G[6] = cmul(G[4], G[2]); G[7] = cmul(G[4], G[3]);
      PUT(0, C); PUT(8, CH);
#pragma unroll
      for (int i = 1; i < 8; ++i) { cf v = cmul(C, G[i]); PUT(i, v); cf v2 = cmul(CH, G[i]); PUT(8 + i, v2); }
    } else if (gw == 1) {
      const cf z2 = cexp_pos(ar, -ai, 2.f), h1 = cexp_pos(ar, -ai, 128.f);
      cf c2 = cmul(C, cexp_pos(ar, -ai, 256.f)), c4 = cmul(C, cexp_pos(ar, -ai, 512.f));
      const cf z4 = cmul(z2, z2), z6 = cmul(z4, z2);
      cf c3 = cmul(c2, h1);
      PUT(16, c2); { cf v = cmul(c2, z2); PUT(17, v); v = cmul(c2, z4); PUT(18, v); v = cmul(c2, z6); PUT(19, v); }
      PUT(20, c3); { cf v = cmul(c3, z2); PUT(21, v); v = cmul(c3, z4); PUT(22, v); v = cmul(c3, z6); PUT(23, v); }
#pragma unroll
      for (int k = 0; k < 4; ++k) { PUT(24 + 2 * k, c4); cf v = cmul(c4, z4); PUT(25 + 2 * k, v); if (k < 3) c4 = cmul(c4, h1); }
    } else if (gw == 2) {
      const cf h1 = cexp_pos(ar, -ai, 128.f);
      cf c8 = cmul(C, cexp_pos(ar, -ai, 1024.f)), c12 = cmul(C, cexp_pos(ar, -ai, 1536.f));
#pragma unroll
      for (int k = 0; k < 5; ++k) { if (k < 4) { PUT(32 + k, c8); if (k < 3) c8 = cmul(c8, h1); } PUT(36 + k, c12); if (k < 4) c12 = cmul(c12, h1); }
    } else {
      const cf w1 = cexp_pos(ar, ai, 8.f);
      cf F = w1, F8 = cexp_pos(ar, ai, 64.f);
      { cf one; one.r = 1.f; one.i = 0.f; PUT(41, one); }
      PUT(49, F8);
#pragma unroll
      for (int j = 1; j < 8; ++j) { PUT(41 + j, F); F8 = cmul(F8, w1); PUT(49 + j, F8); if (j < 7) F = cmul(F, w1); }
    }
    if (h == 0) STAMP(5);
    GBAR();
    {
      const char* bp = area + (41 + i16) * KG_ROW + gw * 64 + kq * 16;
      const bf16x8 bh = *(const bf16x8*)bp, bl = *(const bf16x8*)(bp + 272);
#pragma unroll
      for (int m = 0; m < 3; ++m) {
        const char* apz = area + (i16 + 16 * m) * KG_ROW + gw * 64 + kq * 16;
        const bf16x8 ah = *(const bf16x8*)apz, al = *(const bf16x8*)(apz + 272);
        acc[m] = __builtin_amdgcn_mfma_f32_16x16x32_bf16(ah, bh, acc[m], 0, 0, 0);
        acc[m] = __builtin_amdgcn_mfma_f32_16x16x32_bf16(ah, bl, acc[m], 0, 0, 0);
        acc[m] = __builtin_amdgcn_mfma_f32_16x16x32_bf16(al, bh, acc[m], 0, 0, 0);
      }
    }
    GBAR();
    if (h == 0) STAMP(6);
  }
  STAMP(2);
  float* tab = (float*)area;
#pragma unroll
  for (int m = 0; m < 3; ++m)
#pragma unroll
    for (int j = 0; j < 4; ++j) tab[gw * 768 + (16 * m + 4 * kq + j) * 16 + i16] = acc[m][j];
  asm volatile("s_waitcnt vmcnt(0)" ::: "memory");
  GBAR();
  STAMP(3);
  if (gt < 164) {
    const f32x4* t4 = (const f32x4*)tab + gt;
    const f32x4 k = (t4[0] + t4[192]) + (t4[384] + t4[576]);
    st16(ws + WS_KT + (size_t)d * 3072 + gt * 16, __builtin_bit_cast(u32x4, k));
  }
  if (g == 0) {
#pragma unroll
    for (int i = 0; i < 2; ++i) {
      const int idx = gt + 256 * i, dd = idx >> 3, gi = idx & 7;
      u32x4 v;
#pragma unroll
      for (int j = 0; j < 4; ++j) {
        const int r1 = 63 - 8 * gi - 2 * j, r0 = r1 - 1;
        const int co = (((dd >> 3) ^ ((r1 >> 1) & 7)) * 8 + (dd & 7)) * 2;
        v[j] = (unsigned)*(const u16*)(ut + r1 * 128 + co) | ((unsigned)*(const u16*)(ut + r0 * 128 + co) << 16);
      }
      st16(ws + WS_UR + (size_t)(d0 + dd) * URROW + (1984 - l0 + 8 * gi) * 2, v);
      if (l0 == 0) { u32x4 z = {0, 0, 0, 0}; st16(ws + WS_UR + (size_t)(d0 + dd) * URROW + 4096 + gi * 16, z); }
    }
  }
  STAMP(4);
}

NOPK __global__ __launch_bounds__(1024, 4) void k_pre(const u16* __restrict__ u, const u16* __restrict__ logA, const u16* __restrict__ Aim,
                                                const u16* __restrict__ Cr, const u16* __restrict__ Ci, const u16* __restrict__ logdt,
                                                const u16* __restrict__ x, const u16* __restrict__ w, const u16* __restrict__ wo, char* __restrict__ ws) {
  extern __shared__ __attribute__((aligned(16))) char lds[];
  const int wave = __builtin_amdgcn_readfirstlane(TID >> 6), b = BID;
  STAMP(0);
  GSTAMP(0, 0, ws);
  if (TID < 32) ((unsigned*)(lds + K1_CTR))[TID] = 0u;
  SYNC();
  const int xcd = b & 7, idx = b >> 3;
  const int mb = (xcd & 3) * 4 + (idx & 3), nb = (xcd >> 2) * 8 + (idx >> 2);
  if (wave < 4) {
    gemm_loader(b, mb, nb, wave, x, w, u, wo, ws, lds);
    STAMP(1);
  } else if (wave < 8) {
    {
      unsigned long long a0, a1;
      asm volatile("s_getpc_b64 s[70:71]\n\ts_add_u32 s70, s70, _Z5k_prePKtS0_S0_S0_S0_S0_S0_S0_S0_Pc@rel32@lo+4\n\ts_addc_u32 s71, s71, _Z5k_prePKtS0_S0_S0_S0_S0_S0_S0_S0_Pc@rel32@hi+12\n\ts_mov_b64 %0, s[70:71]" : "=s"(a0) :: "s70", "s71", "scc");
      asm volatile("s_getpc_b64 s[70:71]\n\ts_add_u32 s70, s70, _Z5k_outPKcPKtS2_Pt@rel32@lo+4\n\ts_addc_u32 s71, s71, _Z5k_outPKcPKtS2_Pt@rel32@hi+12\n\ts_mov_b64 %0, s[70:71]" : "=s"(a1) :: "s70", "s71", "scc");
      const unsigned long long len = a1 - a0 + 2048, off = (unsigned long long)(TID - 256) * 128;
      if (a1 > a0 && off < len) {
        unsigned pf;
        asm volatile("global_load_dword %0, %1, off\n\ts_waitcnt vmcnt(0)" : "=v"(pf) : "v"(a0 + off) : "memory");
      }
    }
    if (nb < 10) gemm_consumer<0>(mb, nb, wave - 4, ws, lds);
    else if (nb == 10) gemm_consumer<1>(mb, nb, wave - 4, ws, lds);
    else gemm_consumer<3>(mb, nb, wave - 4, ws, lds);
  } else {
    aux_waves(b, (wave - 8) >> 2, (wave - 8) & 3, u, logA, Aim, Cr, Ci, logdt, wo, ws, lds);
  }
  GSTAMP(0, 1, ws);
}

constexpr int LDS_CP = 2 * CHIMG;
constexpr int LDS_AP = LDS_CP + 4 * 2304 * 4;
constexpr int LDS_FL = LDS_AP + 4 * 2 * 9 * 64 * 4;
constexpr int LDS_K2 = LDS_FL + 64;

#define GLOAD(dst, voff, sbase, off) asm volatile("global_load_dwordx4 %0, %1, %2 offset:" #off : "=v"(dst) : "v"(voff), "s"(sbase))
DI void exp_pack(const f32x16& s, bf16x8& pa, bf16x8& pb) {
  u32x4 a, b;
#pragma unroll
  for (int j = 0; j < 4; ++j) a[j] = pack2(__builtin_amdgcn_exp2f(s[2 * j]), __builtin_amdgcn_exp2f(s[2 * j + 1]));
#pragma unroll
  for (int j = 0; j < 4; ++j) b[j] = pack2(__builtin_amdgcn_exp2f(s[8 + 2 * j]), __builtin_amdgcn_exp2f(s[8 + 2 * j + 1]));
  pa = __builtin_bit_cast(bf16x8, a); pb = __builtin_bit_cast(bf16x8, b);
}
#define EP2(s_, j_) pack2(__builtin_amdgcn_exp2f((s_)[2 * (j_)]), __builtin_amdgcn_exp2f((s_)[2 * (j_) + 1]))
DI void attn_step(const char*& kp, const char*& vp, unsigned ko, unsigned vo, bf16x8& kc, bf16x8& kn, bf16x8& vca, bf16x8& vcb, bf16x8& vna, bf16x8& vnb,
                  bf16x8 q0, bf16x8 q1, const f32x16& nm0, const f32x16& nm1, f32x16& s0, f32x16& o0, f32x16& o1) {
  u32x4 pa, pb;
  pa[0] = EP2(s0, 0); pa[1] = EP2(s0, 1);
  SBAR();
  f32x16 s1 = MFMA32(kc, q1, nm1);
  SBAR();
  GLOAD(vna, vo, vp, 1152); GLOAD(vnb, vo, vp, 1728);
  GLOAD(kc, ko, kp, 2048);
  kp += 1024; vp += 2 * VSTEP;
  SBAR();
  pa[2] = EP2(s0, 2); pa[3] = EP2(s0, 3);
  asm volatile("s_waitcnt vmcnt(3)" : "+v"(vca), "+v"(vcb), "+v"(kn));
  SBAR();
  o0 = MFMA32(vca, __builtin_bit_cast(bf16x8, pa), o0);
  SBAR();
  pb[0] = EP2(s0, 4); pb[1] = EP2(s0, 5); pb[2] = EP2(s0, 6); pb[3] = EP2(s0, 7);
  SBAR();
  o0 = MFMA32(vcb, __builtin_bit_cast(bf16x8, pb), o0);
  SBAR();
  pa[0] = EP2(s1, 0); pa[1] = EP2(s1, 1);
  SBAR();
  s0 = MFMA32(kn, q0, nm0);
  SBAR();
  pa[2] = EP2(s1, 2); pa[3] = EP2(s1, 3);
  SBAR();
  o1 = MFMA32(vca, __builtin_bit_cast(bf16x8, pa), o1);
  SBAR();
  pb[0] = EP2(s1, 4); pb[1] = EP2(s1, 5); pb[2] = EP2(s1, 6); pb[3] = EP2(s1, 7);
  SBAR();
  o1 = MFMA32(vcb, __builtin_bit_cast(bf16x8, pb), o1);
  SBAR();
}
#define ATTN_FIRST_LOADS() do { GLOAD(kA, ko, kp, 0); GLOAD(vAa, vo, vp, 0); GLOAD(vAb, vo, vp, 576); GLOAD(kB, ko, kp, 1024); } while (0)
DI void attn_main(const char* kp, const char* vp, unsigned ko, unsigned vo, bf16x8 q0, bf16x8 q1, const f32x16& nm0, const f32x16& nm1, f32x16& o0, f32x16& o1,
                  bf16x8& kA, bf16x8& kB, bf16x8& vAa, bf16x8& vAb) {
#pragma unroll
  for (int i = 0; i < 16; ++i) { o0[i] = 0.f; o1[i] = 0.f; }
  bf16x8 vBa, vBb;
  asm volatile("" : "+v"(q0), "+v"(q1));
  asm volatile("s_waitcnt vmcnt(3)" : "+v"(kA));
  f32x16 s0 = MFMA32(kA, q0, nm0);
  for (int it = 0; it < 16; ++it) {
    attn_step(kp, vp, ko, vo, kA, kB, vAa, vAb, vBa, vBb, q0, q1, nm0, nm1, s0, o0, o1);
    attn_step(kp, vp, ko, vo, kB, kA, vBa, vBb, vAa, vAb, q0, q1, nm0, nm1, s0, o0, o1);
  }
  asm volatile("s_waitcnt vmcnt(0)" : "+v"(kA), "+v"(kB), "+v"(vAa), "+v"(vAb), "+v"(vBa), "+v"(vBb));
}

NOPK __global__ __launch_bounds__(768, 3) void k_attn_conv(char* __restrict__ ws) {
  extern __shared__ __attribute__((aligned(16))) char lds[];
  const int tid = TID, wave = __builtin_amdgcn_readfirstlane(tid >> 6), lane = tid & 63, r = lane & 31, hh = lane >> 5;
  const int b = BID;
  const int h = (b & 7) * 4 + ((b >> 3) & 3), qt = b >> 5;
#define STAMP2(i)
  STAMP2(0);
  GSTAMP(1, 0, ws);
  const int qg = wave & 3, kh = (wave >> 2) & 1;
  bf16x8 q0, q1; f32x16 nm0, nm1, o0, o1;
  const char* kp = ws + WS_K + (size_t)(h * 64 + kh * 32) * 1024;
  const char* vp = ws + WS_V + (size_t)h * VHEAD + (size_t)(kh * 64) * VSTEP;
  const unsigned ko = lane * 16, vo = hh * 288 + (r < 17 ? r : 17) * 16;
  const int qtile = qt * 8 + qg * 2;
  const int cw = wave - 8, cch = cw >> 1, chalf = cw & 1;
  float cinv0 = 1.f, cinv1 = 1.f;
  bf16x8 kA, kB, vAa, vAb;
  f32x2 kmv; float n0, n1;
  if (wave < 8) {
    q0 = *(const bf16x8*)(ws + WS_Q + ((size_t)(h * 64 + qtile) * 64 + lane) * 16);
    q1 = *(const bf16x8*)(ws + WS_Q + ((size_t)(h * 64 + qtile + 1) * 64 + lane) * 16);
    kmv = ((const f32x2*)(ws + WS_KM))[h * 64 + lane];
    SBAR();
    ATTN_FIRST_LOADS();
  }
  if (tid >= 512 && tid < 528) ((unsigned*)(lds + LDS_FL))[tid - 512] = 0u;
  asm volatile("s_waitcnt lgkmcnt(0)" ::: "memory");
  __builtin_amdgcn_s_barrier();
  asm volatile("" ::: "memory");
  if (wave < 8) {
    n0 = 0.f; n1 = 0.f;
#pragma unroll
    for (int j = 0; j < 4; ++j) {
      const unsigned a = __builtin_bit_cast(u32x4, q0)[j], c = __builtin_bit_cast(u32x4, q1)[j];
      n0 = __builtin_fmaf(bf_lo(a), bf_lo(a), __builtin_fmaf(bf_hi(a), bf_hi(a), n0));
      n1 = __builtin_fmaf(bf_lo(c), bf_lo(c), __builtin_fmaf(bf_hi(c), bf_hi(c), n1));
    }
    n0 = add_other_half(n0); n1 = add_other_half(n1);
    float km = row16_max(__builtin_fmaxf(kmv[0], kmv[1]));
    km = __builtin_fmaxf(__builtin_fmaxf(__builtin_bit_cast(float, __builtin_amdgcn_readlane(__builtin_bit_cast(int, km), 0)), __builtin_bit_cast(float, __builtin_amdgcn_readlane(__builtin_bit_cast(int, km), 16))),
               __builtin_fmaxf(__builtin_bit_cast(float, __builtin_amdgcn_readlane(__builtin_bit_cast(int, km), 32)), __builtin_bit_cast(float, __builtin_amdgcn_readlane(__builtin_bit_cast(int, km), 48))));
    const float m0 = __builtin_amdgcn_sqrtf(n0 * km) * 1.01f, m1 = __builtin_amdgcn_sqrtf(n1 * km) * 1.01f;
#pragma unroll
    for (int i = 0; i < 16; ++i) { nm0[i] = -m0; nm1[i] = -m1; }
  } else {
    __builtin_amdgcn_s_setprio(3);
    const int ct = chalf * 64 + lane;
    char* img = lds + cch * CHIMG;
    u16* tabl = (u16*)(lds + LDS_CP + cch * 2 * 2304 * 4);
    unsigned* mxs = (unsigned*)(lds + LDS_FL) + 12 + cch * 2;
    const char* ur = ws + WS_UR + (size_t)(2 * b + cch) * URROW;
    const u32x4* kt = (const u32x4*)(ws + WS_KT + (size_t)(2 * b + cch) * 3072);
    u32x4 tv0 = kt[ct], tv1 = {0, 0, 0, 0};
    if (ct < 36) tv1 = kt[128 + ct];
    u32x4 ua[3]; u32x2 ub[3]; u32x4 mp[2];
#pragma unroll
    for (int i = 0; i < 3; ++i) {
      const int g = ct + 128 * i;
      ua[i] = u32x4{0, 0, 0, 0}; ub[i] = u32x2{0, 0};
      if (g < 260) { ua[i] = *(const u32x4*)(ur + g * 16); ub[i] = *(const u32x2*)(ur + g * 16 + 16); }
    }
#pragma unroll
    for (int i = 0; i < 2; ++i) mp[i] = *(const u32x4*)(KMAP.v + (ct + 128 * i) * 8);
    { u32x4 z = {0, 0, 0, 0};
      *(u32x4*)(img + UIMG + ct * 16) = z; if (ct < 32) *(u32x4*)(img + UIMG + 2048 + ct * 16) = z; }
    unsigned mu = 0, mk = 0;
#pragma unroll
    for (int i = 0; i < 3; ++i)
#pragma unroll
      for (int j = 0; j < 4; ++j) { const unsigned a = ua[i][j] & 0x7fff7fffu; mu = UMAX(mu, UMAX(a << 16, a & 0xffff0000u)); }
#pragma unroll
    for (int j = 0; j < 4; ++j) mk = UMAX(mk, UMAX(tv0[j] & 0x7fffffffu, tv1[j] & 0x7fffffffu));
    mu = F2U(row16_max(U2F(mu & 0x7f800000u))); mk = F2U(row16_max(U2F(mk & 0x7f800000u)));
    if ((lane & 15) == 0) {
      asm volatile("ds_max_u32 %0, %1\n\tds_max_u32 %0, %2 offset:4" :: "v"((unsigned)(size_t)mxs), "v"(mu), "v"(mk) : "memory");
    }
    lds_barrier((unsigned*)(lds + LDS_FL) + 8 + cch, 2);
    const int eu = (int)(mxs[0] >> 23), ek = (int)(mxs[1] >> 23);
    const int su_e = 268 - eu > 253 ? 253 : 268 - eu, sk_e = 268 - ek > 253 ? 253 : 268 - ek;
    const float su = U2F((unsigned)su_e << 23), sk = U2F((unsigned)sk_e << 23);
    cinv0 = U2F((unsigned)(254 - su_e) << 23); cinv1 = U2F((unsigned)(254 - sk_e) << 23);
#define CVH(x_) __builtin_bit_cast(unsigned, __builtin_amdgcn_cvt_pkrtz(bf_lo(x_) * su, bf_hi(x_) * su))
#pragma unroll
    for (int i = 0; i < 3; ++i) {
      const int g = ct + 128 * i;
      if (g < 260) {
        const unsigned h0 = CVH(ua[i][0]), h1 = CVH(ua[i][1]), h2 = CVH(ua[i][2]), h3 = CVH(ua[i][3]), h4 = CVH(ub[i][0]), h5 = CVH(ub[i][1]);
        const unsigned a0 = __builtin_amdgcn_alignbit(h1, h0, 16), a1 = __builtin_amdgcn_alignbit(h2, h1, 16), a2 = __builtin_amdgcn_alignbit(h3, h2, 16),
                       a3 = __builtin_amdgcn_alignbit(h4, h3, 16), a4 = __builtin_amdgcn_alignbit(h5, h4, 16);
        const u32x4 c0 = {h0, h1, h2, h3}, c1 = {a0, a1, a2, a3}, c2 = {h1, h2, h3, h4}, c3 = {a1, a2, a3, a4};
        *(u32x4*)(img + g * 16) = c0; *(u32x4*)(img + UCOPY + g * 16) = c1;
        *(u32x4*)(img + 2 * UCOPY + g * 16) = c2; *(u32x4*)(img + 3 * UCOPY + g * 16) = c3;
      }
    }
    {
      typedef _Float16 h2_t __attribute__((ext_vector_type(2)));
      u32x2 t0, t1;
#pragma unroll
      for (int j = 0; j < 2; ++j) {
        h2_t a = {(_Float16)(U2F(tv0[2 * j]) * sk), (_Float16)(U2F(tv0[2 * j + 1]) * sk)}, c = {(_Float16)(U2F(tv1[2 * j]) * sk), (_Float16)(U2F(tv1[2 * j + 1]) * sk)};
        t0[j] = __builtin_bit_cast(unsigned, a); t1[j] = __builtin_bit_cast(unsigned, c);
      }
      *(u32x2*)(tabl + ct * 4) = t0;
      if (ct < 64) *(u32x2*)(tabl + 512 + ct * 4) = t1;
    }
    lds_barrier((unsigned*)(lds + LDS_FL) + 8 + cch, 4);
#pragma unroll
    for (int i = 0; i < 2; ++i) {
      const int g = ct + 128 * i;
      u32x4 vh;
#pragma unroll
      for (int j = 0; j < 4; ++j) vh[j] = (unsigned)tabl[mp[i][j] & 0xffff] | ((unsigned)tabl[mp[i][j] >> 16] << 16);
      *(u32x4*)(img + UIMG + (32 + (g >> 2)) * KROW + (g & 3) * 16) = vh;
    }
    lds_barrier((unsigned*)(lds + LDS_FL) + 8 + cch, 6);
    __builtin_amdgcn_s_setprio(0);
  }
  STAMP2(1);
  float* ap = (float*)(lds + LDS_AP);
  volatile int* fl = (volatile int*)(lds + LDS_FL);
  for (int pass = 0;; ++pass) {
    if (wave < 8) {
      attn_main(kp, vp, ko, vo, q0, q1, nm0, nm1, o0, o1, kA, kB, vAa, vAb);
      o0[8] = add_other_half(o0[8]); o1[8] = add_other_half(o1[8]);
      if (kh == 1) {
#pragma unroll
        for (int i = 0; i < 9; ++i) { ap[((qg * 2 + 0) * 9 + i) * 64 + lane] = o0[i]; ap[((qg * 2 + 1) * 9 + i) * 64 + lane] = o1[i]; }
      }
      const bool bad = !(o0[8] >= 8.2718061e-25f) || !(o1[8] >= 8.2718061e-25f);
      const int anybad = (__builtin_amdgcn_ballot_w64(bad) != 0);
      if (lane == 0) fl[wave] = anybad;
    } else if (pass == 0) {
      const int c = (3 - r) & 3;
      const char* ua = lds + cch * CHIMG + c * UCOPY + 2 * (2047 + 8 * hh - r - c);
      const char* kb = lds + cch * CHIMG + UIMG + (r + 32) * KROW + 16 * hh;
      f32x16 a0, a1;
#pragma unroll
      for (int i = 0; i < 16; ++i) { a0[i] = 0.f; a1[i] = 0.f; }
#pragma unroll 2
      for (int dd = 0; dd < 16; ++dd) {
        const int dl = chalf * 16 + dd;
#pragma unroll
        for (int s = 0; s < 2; ++s) {
          const char* up = ua - 64 * dl + 32 * s;
          const s16x4 x0 = *(const s16x4*)up, x1 = *(const s16x4*)(up + 8);
          const s16x4 y0 = *(const s16x4*)(up - 2048), y1 = *(const s16x4*)(up - 2048 + 8);
          const bf16x8 A0 = __builtin_shufflevector(x0, x1, 0, 1, 2, 3, 4, 5, 6, 7), A1 = __builtin_shufflevector(y0, y1, 0, 1, 2, 3, 4, 5, 6, 7);
          const char* kq = kb - KROW * dl + 32 * s;
          const bf16x8 B0 = *(const bf16x8*)kq, B1 = *(const bf16x8*)(kq + 32 * KROW);
          a0 = MFMA32H(A0, B0, a0); a1 = MFMA32H(A0, B1, a1);
          a1 = MFMA32H(A1, B0, a1);
        }
      }
      float* cp = (float*)(lds + LDS_CP) + cw * 2304;
#pragma unroll
      for (int g = 0; g < 4; ++g) {
        f32x4 v0 = {a0[4 * g], a0[4 * g + 1], a0[4 * g + 2], a0[4 * g + 3]}, v1 = {a1[4 * g], a1[4 * g + 1], a1[4 * g + 2], a1[4 * g + 3]};
        *(f32x4*)(cp + 36 * r + 8 * g + 4 * hh) = v0;
        *(f32x4*)(cp + 36 * (32 + r) + 8 * g + 4 * hh) = v1;
      }
      lds_barrier((unsigned*)(lds + LDS_FL) + 8 + cch, 8);
#pragma unroll
      for (int i = 0; i < 2; ++i) {
        const int g = chalf * 128 + i * 64 + lane;
        const float* c0 = (const float*)(lds + LDS_CP) + (2 * cch) * 2304 + 36 * (g >> 2) + 8 * (g & 3);
        const f32x4 a = (*(const f32x4*)c0 + *(const f32x4*)(c0 + 2304)) * cinv0 * cinv1, bq = (*(const f32x4*)(c0 + 4) + *(const f32x4*)(c0 + 2304 + 4)) * cinv0 * cinv1;
        u32x4 w = {pack2(a[0], a[1]), pack2(a[2], a[3]), pack2(bq[0], bq[1]), pack2(bq[2], bq[3])};
        st16(ws + WS_YT + ((size_t)(2 * b + cch) * L + 8 * g) * 2, w);
      }
    }
    STAMP2(2);
    SYNC();
    STAMP2(3);
    int redo = 0;
    if (pass == 0) {
#pragma unroll
      for (int i = 0; i < 8; ++i) redo |= fl[i];
    }
    if (!redo) break;
    if (wave < 8) {
      const char* kall = ws + WS_K + ((size_t)(h * 64) * 64 + lane) * 16;
      float m0 = -3.0e38f, m1 = -3.0e38f;
      f32x16 z;
#pragma unroll
      for (int i = 0; i < 16; ++i) z[i] = 0.f;
      for (int it = 0; it < 64; ++it) {
        const bf16x8 kf = *(const bf16x8*)(kall + it * 1024);
        const f32x16 s0 = MFMA32(kf, q0, z), s1 = MFMA32(kf, q1, z);
#pragma unroll
        for (int i = 0; i < 16; ++i) { m0 = __builtin_fmaxf(m0, s0[i]); m1 = __builtin_fmaxf(m1, s1[i]); }
      }
      m0 = max_other_half(m0); m1 = max_other_half(m1);
#pragma unroll
      for (int i = 0; i < 16; ++i) { nm0[i] = -m0; nm1[i] = -m1; }
      ATTN_FIRST_LOADS();
    }
    SYNC();
  }
  if (wave < 4) {
#pragma unroll
    for (int t = 0; t < 2; ++t) {
      float v[9];
#pragma unroll
      for (int i = 0; i < 9; ++i) v[i] = (t ? o1[i] : o0[i]) + ap[((qg * 2 + t) * 9 + i) * 64 + lane];
      const float inv = 1.f / v[8];
      u32x4 w;
#pragma unroll
      for (int j = 0; j < 4; ++j) w[j] = pack2(v[2 * j] * inv, v[2 * j + 1] * inv);
      st16(ws + WS_O + ((size_t)((qtile + t) * 32 + h) * 64 + lane) * 16, w);
    }
  }
  STAMP2(4);
  GSTAMP(1, 1, ws);
}

constexpr int RED_STRIDE = 68;
DI void out_role(int b, const char* ws, u16* out, char* lds) {
  const int tid = TID, wave = tid >> 6, lane = tid & 63, r = lane & 31, hh = lane >> 5;
  const int xcd = b & 7, idx = b >> 3, mt = xcd * 4 + (idx & 3), nt = idx >> 2;
  bf16x8 of[2][8], wf[2][8];
#pragma unroll
  for (int k = 0; k < 8; ++k) {
    const int h = wave * 8 + k;
#pragma unroll
    for (int i = 0; i < 2; ++i) {
      of[i][k] = *(const bf16x8*)(ws + WS_O + ((size_t)((2 * mt + i) * 32 + h) * 64 + lane) * 16);
      wf[i][k] = *(const bf16x8*)(ws + WS_WO + ((size_t)((2 * nt + i) * 32 + h) * 64 + lane) * 16);
    }
  }
  f32x16 acc[2][2];
#pragma unroll
  for (int i = 0; i < 2; ++i)
#pragma unroll
    for (int j = 0; j < 2; ++j)
#pragma unroll
      for (int e = 0; e < 16; ++e) acc[i][j][e] = 0.f;
#pragma unroll
  for (int k = 0; k < 8; ++k)
#pragma unroll
    for (int i = 0; i < 2; ++i)
#pragma unroll
      for (int j = 0; j < 2; ++j) acc[i][j] = MFMA32(of[i][k], wf[j][k], acc[i][j]);
  float* red = (float*)lds + wave * 64 * RED_STRIDE;
#pragma unroll
  for (int i = 0; i < 2; ++i)
#pragma unroll
    for (int j = 0; j < 2; ++j)
#pragma unroll
      for (int e = 0; e < 16; ++e) red[(32 * i + crow(e, hh)) * RED_STRIDE + 32 * j + r] = acc[i][j][e];
  SYNC();
  const int tok = tid >> 2, n0 = (tid & 3) * 16;
  const float* rp = (const float*)lds + tok * RED_STRIDE + n0;
  u32x4 w[2];
#pragma unroll
  for (int g = 0; g < 4; ++g) {
    f32x4 s = *(const f32x4*)(rp + 4 * g);
#pragma unroll
    for (int wv = 1; wv < 4; ++wv) s += *(const f32x4*)(rp + wv * 64 * RED_STRIDE + 4 * g);
    w[g >> 1][(g & 1) * 2] = pack2(s[0], s[1]); w[g >> 1][(g & 1) * 2 + 1] = pack2(s[2], s[3]);
  }
  u16* op = out + (size_t)(mt * 64 + tok) * 1024 + 512 + nt * 64 + n0;
  *(u32x4*)op = w[0]; *(u32x4*)(op + 8) = w[1];
}
DI void fin_role(int b, const char* ws, const u16* u, const u16* dsk, u16* out, char* lds) {
  const int tid = TID;
  const int l0 = (b >> 3) * 64, d0 = (b & 7) * 64;
  u16* t = (u16*)lds;
#pragma unroll
  for (int i = 0; i < 2; ++i) {
    const int idx = tid + 256 * i, row = idx >> 3, ch = idx & 7;
    *(u32x4*)(t + row * 72 + ch * 8) = *(const u32x4*)(ws + WS_YT + ((size_t)(d0 + row) * L + l0 + ch * 8) * 2);
  }
  SYNC();
#pragma unroll
  for (int i = 0; i < 2; ++i) {
    const int idx = tid + 256 * i, ll = idx >> 3, ch = idx & 7;
    const u32x4 uv = *(const u32x4*)(u + (size_t)(l0 + ll) * DM + d0 + ch * 8);
    const u32x4 dv = *(const u32x4*)(dsk + d0 + ch * 8);
    u32x4 v;
#pragma unroll
    for (int j = 0; j < 4; ++j) {
      const float y0 = bf2f(t[(ch * 8 + 2 * j) * 72 + ll]), y1 = bf2f(t[(ch * 8 + 2 * j + 1) * 72 + ll]);
      const unsigned sk = pack2(bf_lo(uv[j]) * bf_lo(dv[j]), bf_hi(uv[j]) * bf_hi(dv[j]));
      v[j] = pack2(y0 + bf_lo(sk), y1 + bf_hi(sk));
    }
    *(u32x4*)(out + (size_t)(l0 + ll) * 1024 + d0 + ch * 8) = v;
  }
}
NOPK __global__ __launch_bounds__(256) void k_out(const char* ws, const u16* u, const u16* dsk, u16* out) {
  extern __shared__ __attribute__((aligned(16))) char lds[];
  GSTAMP(2, 0, ws);
  if (BID < 256) out_role(BID, ws, out, lds);
  else fin_role(BID - 256, ws, u, dsk, out, lds);
  GSTAMP(2, 1, ws);
}

extern "C" void kernel_launch(void* const* d_in, const int* in_sizes, int n_in, void* d_out, int out_size, void* d_ws, size_t ws_size,
                              hipStream_t stream) {
  const u16* u = (const u16*)d_in[0]; const u16* logA = (const u16*)d_in[1]; const u16* Aim = (const u16*)d_in[2];
  const u16* Cr = (const u16*)d_in[3]; const u16* Ci = (const u16*)d_in[4]; const u16* logdt = (const u16*)d_in[5];
  const u16* dsk = (const u16*)d_in[6]; const u16* x = (const u16*)d_in[7]; const u16* wqkv = (const u16*)d_in[8]; const u16* wo = (const u16*)d_in[9];
  u16* out = (u16*)d_out; char* ws = (char*)d_ws;
  constexpr int LDS_OUT = 4 * 64 * RED_STRIDE * 4;
  (void)hipFuncSetAttribute((const void*)k_pre, hipFuncAttributeMaxDynamicSharedMemorySize, LDS_K1);
  (void)hipFuncSetAttribute((const void*)k_attn_conv, hipFuncAttributeMaxDynamicSharedMemorySize, LDS_K2);
  (void)hipFuncSetAttribute((const void*)k_out, hipFuncAttributeMaxDynamicSharedMemorySize, LDS_OUT);
  k_pre<<<256, 1024, LDS_K1, stream>>>(u, logA, Aim, Cr, Ci, logdt, x, wqkv, wo, ws);
  k_attn_conv<<<256, 768, LDS_K2, stream>>>(ws);
  k_out<<<512, 256, LDS_OUT, stream>>>(ws, u, dsk, out);
}
```

```cpp
#include <hip/hip_runtime.h>
#include <hip/hip_bf16.h>

#define TID ((int)__builtin_amdgcn_workitem_id_x())
#define BID ((int)__builtin_amdgcn_workgroup_id_x())
#define SYNC() do { __builtin_amdgcn_fence(__ATOMIC_RELEASE, "workgroup"); __builtin_amdgcn_s_barrier(); __builtin_amdgcn_fence(__ATOMIC_ACQUIRE, "workgroup"); } while (0)
#define UMAX(a, b) ((a) > (b) ? (a) : (b))
#define U2F(x) __builtin_bit_cast(float, (unsigned)(x))
#define F2U(x) __builtin_bit_cast(unsigned, (float)(x))
typedef unsigned short u16;
typedef short bf16x8 __attribute__((ext_vector_type(8)));
typedef short s16x4 __attribute__((ext_vector_type(4)));
typedef float f32x16 __attribute__((ext_vector_type(16)));
typedef float f32x4 __attribute__((ext_vector_type(4)));
typedef float f32x2 __attribute__((ext_vector_type(2)));
typedef unsigned u32x4 __attribute__((ext_vector_type(4)));
typedef unsigned u32x2 __attribute__((ext_vector_type(2)));
typedef __bf16 bf16x2_t __attribute__((ext_vector_type(2)));
typedef _Float16 f16x8 __attribute__((ext_vector_type(8)));
#define MFMA32H(a, b, c) __builtin_amdgcn_mfma_f32_32x32x16_f16(__builtin_bit_cast(f16x8, a), __builtin_bit_cast(f16x8, b), (c), 0, 0, 0)
typedef __attribute__((address_space(3))) s16x4* lds_s16x4_ptr;
#if defined(__HIP_DEVICE_COMPILE__)
#define NOPK __attribute__((target("no-packed-fp32-ops")))
#else
#define NOPK
#endif
#define DI NOPK __device__ __forceinline__
#define MFMA32(a, b, c) __builtin_amdgcn_mfma_f32_32x32x16_bf16((a), (b), (c), 0, 0, 0)
#define SBAR() __builtin_amdgcn_sched_barrier(0)
#define GLDS16(src, dst) __builtin_amdgcn_global_load_lds((const unsigned*)(src), (unsigned*)(dst), 16, 0, 0)

constexpr int L = 2048, DM = 512, NS = 128, NH = 32;
constexpr size_t MB = 1024 * 1024;
constexpr size_t WS_Q = 0, WS_K = 2 * MB, WS_V = 4 * MB, WS_QN = 6 * MB + 512 * 1024, WS_KM = 6 * MB + 768 * 1024,
                 WS_O = 7 * MB, WS_WO = 9 * MB, WS_YT = 10 * MB, WS_UR = 12 * MB, WS_KT = 15 * MB;
constexpr int URROW = 4224;
constexpr int VSTEP = 576;
constexpr int VHEAD = 128 * VSTEP;
constexpr float QSCALE = 0.25f * 1.4426950408889634f;
constexpr int UCOPY = 4160, UIMG = 4 * UCOPY, KROW = 80, KIMG = 96 * KROW, CHIMG = UIMG + KIMG;

DI unsigned pack2(float a, float b) { f32x2 v = {a, b}; bf16x2_t r = __builtin_convertvector(v, bf16x2_t); return __builtin_bit_cast(unsigned, r); }
DI void st16(void* p, u32x4 v) { asm volatile("global_store_dwordx4 %0, %1, off sc1\n\ts_nop 1" :: "v"(p), "v"(v) : "memory"); }
DI void st4(void* p, unsigned v) { asm volatile("global_store_dword %0, %1, off sc1" :: "v"(p), "v"(v) : "memory"); }
DI float bf_lo(unsigned p) { return U2F(p << 16); }
DI float bf_hi(unsigned p) { return U2F(p & 0xffff0000u); }
DI float bf2f(u16 v) { return U2F(((unsigned)v) << 16); }
DI float bfr(float x) { return bf_lo(pack2(x, 0.f)); }
DI int crow(int reg, int hh) { return (reg & 3) + 8 * (reg >> 2) + 4 * hh; }
template <int CTRL> DI float dppf(float x) { return __builtin_bit_cast(float, __builtin_amdgcn_update_dpp(0, __builtin_bit_cast(int, x), CTRL, 0xf, 0xf, false)); }
DI float add_other_half(float x) { auto rr = __builtin_amdgcn_permlane32_swap(F2U(x), F2U(x), false, false); return U2F(rr[0]) + U2F(rr[1]); }
DI float max_other_half(float x) { auto rr = __builtin_amdgcn_permlane32_swap(F2U(x), F2U(x), false, false); return __builtin_fmaxf(U2F(rr[0]), U2F(rr[1])); }
DI float row16_max(float x) { x = __builtin_fmaxf(x, dppf<0x128>(x)); x = __builtin_fmaxf(x, dppf<0x124>(x)); x = __builtin_fmaxf(x, dppf<0x122>(x)); x = __builtin_fmaxf(x, dppf<0x121>(x)); return x; }

DI void lds_barrier(unsigned* ctr, unsigned target) {
  const unsigned addr = (unsigned)(size_t)ctr;
  asm volatile("s_waitcnt lgkmcnt(0)" ::: "memory");
  if ((TID & 63) == 0) asm volatile("ds_add_u32 %0, %1" :: "v"(addr), "v"(1u) : "memory");
  for (;;) {
    unsigned v;
    asm volatile("ds_read_b32 %0, %1\n\ts_waitcnt lgkmcnt(0)" : "=v"(v) : "v"(addr) : "memory");
    if ((unsigned)__builtin_amdgcn_readfirstlane(v) >= target) break;
    __builtin_amdgcn_s_sleep(1);
  }
}

struct MapT { u16 v[2048]; };
constexpr MapT make_map() {
  MapT m{};
  for (int l = 0; l < 2048; ++l) {
    int P = l;
    if (l >= 256) {
      const int s = l >= 1024 ? 3 : (l >= 512 ? 2 : 1);
      int q = l >> s; const int rem = l & ((1 << s) - 1), half = 1 << (s - 1);
      if (rem > half || (rem == half && (q & 1))) ++q;
      P = q << s;
    }
    const int rem = P & 127, lo = rem >> 3, f = rem & 7, b = P >> 7;
    int i = 0;
    if (P < 256) i = 8 * b + f;
    else if (P < 512) i = 16 + 4 * (b - 2) + (f >> 1);
    else if (P < 1024) i = 24 + 2 * (b - 4) + (f >> 2);
    else i = 32 + (b - 8);
    m.v[l] = (u16)(i * 16 + lo);
  }
  return m;
}
__device__ const MapT KMAP = make_map();

constexpr int K1_W = 0, K1_X = 36864, K1_AUX = 86016, KG_ROW = 544, KG_AREA = 57 * KG_ROW, K1_UT = K1_AUX + 2 * KG_AREA, K1_CTR = K1_UT + 64 * 72 * 2, LDS_K1 = K1_CTR + 128;

DI void lds_signal(unsigned addr) {
  if ((TID & 63) == 0) asm volatile("ds_add_u32 %0, %1" :: "v"(addr), "v"(1u) : "memory");
}
DI void lds_wait(unsigned addr, unsigned target) {
  for (;;) {
    unsigned v;
    asm volatile("ds_read_b32 %0, %1\n\ts_waitcnt lgkmcnt(0)" : "=v"(v) : "v"(addr) : "memory");
    if ((unsigned)__builtin_amdgcn_readfirstlane(v) >= target) break;
    __builtin_amdgcn_s_sleep(1);
  }
}

DI void gemm_loader(int b, int mb, int nb, int wave, const u16* __restrict__ x, const u16* __restrict__ w, const u16* __restrict__ u,
                    const u16* __restrict__ wo, char* __restrict__ ws, char* lds) {
  const int lane = TID & 63;
  const int tok0 = mb * 128 + wave * 32;
  const u16* wsrc[3]; const u16* xsrc[4];
#pragma unroll
  for (int i = 0; i < 3; ++i) { const int idx = (wave * 3 + i) * 64 + lane, row = idx / 12, ch = idx - row * 12; wsrc[i] = w + (size_t)row * 1536 + nb * 96 + ch * 8; }
#pragma unroll
  for (int j = 0; j < 4; ++j) { const int idx = j * 64 + lane, row = idx >> 3, ch = (idx & 7) ^ ((row >> 1) & 7); xsrc[j] = x + (size_t)(tok0 + row) * 512 + ch * 8; }
  char* wdst = lds + K1_W + (wave * 3 * 64 + lane) * 16;
  char* xdst = lds + K1_X + wave * 12288 + lane * 16;
  const unsigned cfull = (unsigned)(size_t)(lds + K1_CTR), cdone = cfull + 32;
  int slot = 0;
#pragma nounroll
  for (int c = 0; c < 8; ++c) {
    if (c >= 2) {
      asm volatile("s_waitcnt vmcnt(7)" ::: "memory");
      lds_signal(cfull + 4 * (c - 2));
      if (c >= 3) lds_wait(cdone + 4 * (c - 3), 4);
    }
#pragma unroll
    for (int i = 0; i < 3; ++i) { GLDS16(wsrc[i], wdst + slot * 12288 + i * 1024); wsrc[i] += 64 * 1536; }
#pragma unroll
    for (int j = 0; j < 4; ++j) { GLDS16(xsrc[j], xdst + slot * 4096 + j * 1024); xsrc[j] += 64; }
    slot = slot == 2 ? 0 : slot + 1;
  }
  asm volatile("s_waitcnt vmcnt(7)" ::: "memory"); lds_signal(cfull + 24);
  asm volatile("s_waitcnt vmcnt(0)" ::: "memory"); lds_signal(cfull + 28);
}

template <int NV>
DI void gemm_consumer(int mb, int nb, int wave, char* __restrict__ ws, char* lds) {
  const int lane = TID & 63, r = lane & 31, hh = lane >> 5;
  const int tok0 = mb * 128 + wave * 32;
  f32x16 acc[3];
#pragma unroll
  for (int t = 0; t < 3; ++t)
#pragma unroll
    for (int i = 0; i < 16; ++i) acc[t][i] = 0.f;
  const int q = (lane & 15) >> 2, p = lane & 3, blk = (lane >> 4) & 1;
  const char* tb = lds + K1_W + (8 * hh + q) * 192 + (16 * blk + 4 * p) * 2;
  const char* xb = lds + K1_X + wave * 12288 + r * 128;
  const int xsw = (r >> 1) & 7;
  const unsigned cfull = (unsigned)(size_t)(lds + K1_CTR), cdone = cfull + 32;
  bf16x8 xa[2], wa[2][3], xbf[2], wbf[2][3];
#define RD_HALF(XF, WF, slot_, k0_) do { const char* wp_ = tb + (slot_) * 12288; const char* xp_ = xb + (slot_) * 4096; \
    _Pragma("unroll") for (int ks = 0; ks < 2; ++ks) { \
      XF[ks] = *(const bf16x8*)(xp_ + (((2 * ((k0_) + ks) + hh) ^ xsw) * 16)); \
      _Pragma("unroll") for (int t = 0; t < 3; ++t) { const char* a_ = wp_ + ((k0_) + ks) * 16 * 192 + t * 64; \
        s16x4 lo_ = __builtin_amdgcn_ds_read_tr16_b64_v4i16((lds_s16x4_ptr)a_); \
        s16x4 hi_ = __builtin_amdgcn_ds_read_tr16_b64_v4i16((lds_s16x4_ptr)(a_ + 4 * 192)); \
        WF[ks][t] = __builtin_shufflevector(lo_, hi_, 0, 1, 2, 3, 4, 5, 6, 7); } } } while (0)
#define MM_HALF(XF, WF) do { _Pragma("unroll") for (int ks = 0; ks < 2; ++ks) _Pragma("unroll") for (int t = 0; t < 3; ++t) { \
      if (t >= 3 - NV) acc[t] = MFMA32(XF[ks], WF[ks][t], acc[t]); else acc[t] = MFMA32(WF[ks][t], XF[ks], acc[t]); } } while (0)
  int slot = 0;
  const unsigned one0 = lane == 0 ? 1u : 0u;
  lds_wait(cfull, 4);
  RD_HALF(xa, wa, 0, 0);
#pragma nounroll
  for (int c = 0; c < 8; ++c) {
    RD_HALF(xbf, wbf, slot, 2);
    MM_HALF(xa, wa);
#pragma unroll
    for (int i = 0; i < 6; ++i) { __builtin_amdgcn_sched_group_barrier(0x8, 1, 0); __builtin_amdgcn_sched_group_barrier(0x100, 3, 0); }
    SBAR();
    unsigned pv;
    asm volatile("s_waitcnt lgkmcnt(0)\n\ts_mov_b64 exec, 1\n\tds_add_u32 %1, %2\n\ts_mov_b64 exec, -1\n\tds_read_b32 %0, %3" : "=&v"(pv) : "v"(cdone + 4 * c), "v"(one0), "v"(cfull + 4 * ((c + 1) & 7)) : "memory");
    slot = slot == 2 ? 0 : slot + 1;
    SBAR();
#pragma unroll
    for (int t = 0; t < 3; ++t) { if (t >= 3 - NV) acc[t] = MFMA32(xbf[0], wbf[0][t], acc[t]); else acc[t] = MFMA32(wbf[0][t], xbf[0], acc[t]); }
    SBAR();
    asm volatile("s_waitcnt lgkmcnt(0)" : "+v"(pv) :: "memory");
    if (c < 7) {
      if ((unsigned)__builtin_amdgcn_readfirstlane(pv) < 4u) lds_wait(cfull + 4 * (c + 1), 4);
      SBAR();
      RD_HALF(xa, wa, slot, 0);
    }
#pragma unroll
    for (int t = 0; t < 3; ++t) { if (t >= 3 - NV) acc[t] = MFMA32(xbf[1], wbf[1][t], acc[t]); else acc[t] = MFMA32(wbf[1][t], xbf[1], acc[t]); }
    SBAR();
  }
  const int tt = tok0 >> 5;
#pragma unroll
  for (int t = 0; t < 3; ++t) {
    const int f0 = nb * 96 + t * 32;
    if (t >= 3 - NV) {
      const int h = ((f0 - 1024) >> 4) + (r >> 4), d = r & 15;
#pragma unroll
      for (int s = 0; s < 2; ++s) {
        u32x4 v;
#pragma unroll
        for (int j = 0; j < 4; ++j) v[j] = pack2(acc[t][8 * s + 2 * j], acc[t][8 * s + 2 * j + 1]);
        char* vb = ws + WS_V + (size_t)h * VHEAD + (2 * tt + s) * VSTEP + hh * 288;
        st16(vb + d * 16, v);
        if (d < 2) { const unsigned o1 = d ? 0u : 0x3f803f80u; u32x4 one = {o1, o1, o1, o1}; st16(vb + 256 + d * 16, one); }
      }
    } else {
      const bool isQ = f0 < 512;
      const float sc = isQ ? QSCALE : 1.f;
#pragma unroll
      for (int a = 0; a < 2; ++a) {
        const int h = ((f0 & 511) >> 4) + a;
        u32x4 v; float n2 = 0.f;
#pragma unroll
        for (int j = 0; j < 4; ++j) {
          const float e0 = acc[t][8 * a + 2 * j] * sc, e1 = acc[t][8 * a + 2 * j + 1] * sc;
          v[j] = pack2(e0, e1);
          n2 = __builtin_fmaf(e0, e0, __builtin_fmaf(e1, e1, n2));
        }
        st16(ws + (isQ ? WS_Q : WS_K) + ((size_t)(h * 64 + tt) * 64 + lane) * 16, v);
        if (!isQ) {
          const float nmx = row16_max(add_other_half(n2));
          if ((lane & 47) == 0) st4((float*)(ws + WS_KM) + (h * 64 + tt) * 2 + (lane >> 4), F2U(nmx));
        }
      }
    }
  }
}

struct cf { float r, i; };
DI cf cmul(cf a, cf b) { cf c; c.r = a.r * b.r - a.i * b.i; c.i = a.r * b.i + a.i * b.r; return c; }
__device__ __noinline__ float exp_precise(float x) { return (float)exp((double)x); }
DI float exp_bf16(float x) {
  float e = __builtin_expf(x);
  const int b = (int)(F2U(e) & 0xffffu) - 0x8000;
  if (__builtin_expect(b >= -4 && b <= 4, 0)) e = exp_precise(x);
  return bfr(e);
}
DI cf cexp_pos(float ar, float ai, float p) {
  const float e = __builtin_amdgcn_exp2f(ar * p * 1.4426950408889634f);
  const float y = ai * p;
  const float c_hi = 0.15915494309189535f, c_lo = (float)(0.15915494309189533577 - (double)0.15915494309189535f);
  const float fr = __builtin_fmaf(y, c_lo, __builtin_fmaf(y, c_hi, -__builtin_rintf(y * c_hi)));
  cf o; o.r = e * __builtin_amdgcn_cosf(fr); o.i = e * __builtin_amdgcn_sinf(fr);
  return o;
}
DI void aux_waves(int b, int g, int gw, const u16* u, const u16* logA, const u16* Aim, const u16* Cr, const u16* Ci, const u16* logdt,
                  const u16* wo, char* ws, char* lds) {
  const int lane = TID & 63, gt = gw * 64 + lane;
  const int d = 2 * b + g;
  unsigned* ctr = (unsigned*)(lds + K1_CTR) + 16 + g;
  unsigned nbar = 0;
#define GBAR() do { nbar += 4; lds_barrier(ctr, nbar); } while (0)
  char* area = lds + K1_AUX + g * KG_AREA;
  const u16 p_dt = logdt[d];
  const int po = d * NS + lane;
  const u16 pa0 = logA[po], pa1 = logA[po + 64], pi0 = Aim[po], pi1 = Aim[po + 64], pr0 = Cr[po], pr1 = Cr[po + 64], pc0 = Ci[po], pc1 = Ci[po + 64];
  const int l0 = (b >> 3) * 64, d0 = (b & 7) * 64;
  char* ut = lds + K1_UT;
  if (g == 0) {
#pragma unroll
    for (int i = 0; i < 2; ++i) { const int idx = gt + 256 * i, row = idx >> 3, ch = (idx & 7) ^ ((row >> 1) & 7); GLDS16(u + (size_t)(l0 + row) * DM + d0 + ch * 8, ut + idx * 16); }
  } else if (gw >= 2) {
    const int gi = b * 128 + gt - 128;
    const int ln = gi & 63, h = (gi >> 6) & 31, nt = gi >> 11, c = ln & 31, hh = ln >> 5;
    u32x4 v;
#pragma unroll
    for (int j = 0; j < 4; ++j) {
      const int dd = (j < 2 ? 4 * hh + 2 * j : 8 + 4 * hh + 2 * (j - 2));
      v[j] = (unsigned)wo[(size_t)(h * 16 + dd) * DM + nt * 32 + c] | ((unsigned)wo[(size_t)(h * 16 + dd + 1) * DM + nt * 32 + c] << 16);
    }
    st16(ws + WS_WO + (size_t)gi * 16, v);
  }
  unsigned* EA = (unsigned*)area + lane;
  const float dt = exp_bf16(bf2f(p_dt));
  f32x4 acc[3];
#pragma unroll
  for (int m = 0; m < 3; ++m) { acc[m][0] = 0.f; acc[m][1] = 0.f; acc[m][2] = 0.f; acc[m][3] = 0.f; }
  const int i16 = lane & 15, kq = lane >> 4;
#define PUT(i_, v_) do { const unsigned hi_ = pack2((v_).r, (v_).i); EA[(i_) * (KG_ROW / 4)] = hi_; EA[(i_) * (KG_ROW / 4) + 68] = pack2((v_).r - bf_lo(hi_), (v_).i - bf_hi(hi_)); } while (0)
#pragma nounroll
  for (int h = 0; h < 2; ++h) {
    const float ar = -exp_bf16(bf2f(h ? pa1 : pa0)) * dt, ai = bf2f(h ? pi1 : pi0) * dt;
    cf C; C.r = 2.f * bf2f(h ? pr1 : pr0) * dt; C.i = -2.f * bf2f(h ? pc1 : pc0) * dt;
    if (gw == 0) {
      cf G[8]; G[1] = cexp_pos(ar, -ai, 1.f);
      const cf CH = cmul(C, cexp_pos(ar, -ai, 128.f));
      G[2] = cmul(G[1], G[1]); G[3] = cmul(G[2], G[1]); G[4] = cmul(G[2], G[2]); G[5] = cmul(G[4], G[1]); G[6] = cmul(G[4], G[2]); G[7] = cmul(G[4], G[3]);
      PUT(0, C); PUT(8, CH);
#pragma unroll
      for (int i = 1; i < 8; ++i) { cf v = cmul(C, G[i]); PUT(i, v); cf v2 = cmul(CH, G[i]); PUT(8 + i, v2); }
    } else if (gw == 1) {
      const cf z2 = cexp_pos(ar, -ai, 2.f), h1 = cexp_pos(ar, -ai, 128.f);
      cf c2 = cmul(C, cexp_pos(ar, -ai, 256.f)), c4 = cmul(C, cexp_pos(ar, -ai, 512.f));
      const cf z4 = cmul(z2, z2), z6 = cmul(z4, z2);
      cf c3 = cmul(c2, h1);
      PUT(16, c2); { cf v = cmul(c2, z2); PUT(17, v); v = cmul(c2, z4); PUT(18, v); v = cmul(c2, z6); PUT(19, v); }
      PUT(20, c3); { cf v = cmul(c3, z2); PUT(21, v); v = cmul(c3, z4); PUT(22, v); v = cmul(c3, z6); PUT(23, v); }
#pragma unroll
      for (int k = 0; k < 4; ++k) { PUT(24 + 2 * k, c4); cf v = cmul(c4, z4); PUT(25 + 2 * k, v); if (k < 3) c4 = cmul(c4, h1); }
    } else if (gw == 2) {
      const cf h1 = cexp_pos(ar, -ai, 128.f);
      cf c8 = cmul(C, cexp_pos(ar, -ai, 1024.f)), c12 = cmul(C, cexp_pos(ar, -ai, 1536.f));
#pragma unroll
      for (int k = 0; k < 5; ++k) { if (k < 4) { PUT(32 + k, c8); if (k < 3) c8 = cmul(c8, h1); } PUT(36 + k, c12); if (k < 4) c12 = cmul(c12, h1); }
    } else {
      const cf w1 = cexp_pos(ar, ai, 8.f);
      cf F = w1, F8 = cexp_pos(ar, ai, 64.f);
      { cf one; one.r = 1.f; one.i = 0.f; PUT(41, one); }
      PUT(49, F8);
#pragma unroll
      for (int j = 1; j < 8; ++j) { PUT(41 + j, F); F8 = cmul(F8, w1); PUT(49 + j, F8); if (j < 7) F = cmul(F, w1); }
    }
    GBAR();
    {
      const char* bp = area + (41 + i16) * KG_ROW + gw * 64 + kq * 16;
      const bf16x8 bh = *(const bf16x8*)bp, bl = *(const bf16x8*)(bp + 272);
#pragma unroll
      for (int m = 0; m < 3; ++m) {
        const char* apz = area + (i16 + 16 * m) * KG_ROW + gw * 64 + kq * 16;
        const bf16x8 ah = *(const bf16x8*)apz, al = *(const bf16x8*)(apz + 272);
        acc[m] = __builtin_amdgcn_mfma_f32_16x16x32_bf16(ah, bh, acc[m], 0, 0, 0);
        acc[m] = __builtin_amdgcn_mfma_f32_16x16x32_bf16(ah, bl, acc[m], 0, 0, 0);
        acc[m] = __builtin_amdgcn_mfma_f32_16x16x32_bf16(al, bh, acc[m], 0, 0, 0);
      }
    }
    GBAR();
  }
  float* tab = (float*)area;
#pragma unroll
  for (int m = 0; m < 3; ++m)
#pragma unroll
    for (int j = 0; j < 4; ++j) tab[gw * 768 + (16 * m + 4 * kq + j) * 16 + i16] = acc[m][j];
  asm volatile("s_waitcnt vmcnt(0)" ::: "memory");
  GBAR();
  if (gt < 164) {
    const f32x4* t4 = (const f32x4*)tab + gt;
    const f32x4 k = (t4[0] + t4[192]) + (t4[384] + t4[576]);
    st16(ws + WS_KT + (size_t)d * 3072 + gt * 16, __builtin_bit_cast(u32x4, k));
  }
  if (g == 0) {
#pragma unroll
    for (int i = 0; i < 2; ++i) {
      const int idx = gt + 256 * i, dd = idx >> 3, gi = idx & 7;
      u32x4 v;
#pragma unroll
      for (int j = 0; j < 4; ++j) {
        const int r1 = 63 - 8 * gi - 2 * j, r0 = r1 - 1;
        const int co = (((dd >> 3) ^ ((r1 >> 1) & 7)) * 8 + (dd & 7)) * 2;
        v[j] = (unsigned)*(const u16*)(ut + r1 * 128 + co) | ((unsigned)*(const u16*)(ut + r0 * 128 + co) << 16);
      }
      st16(ws + WS_UR + (size_t)(d0 + dd) * URROW + (1984 - l0 + 8 * gi) * 2, v);
      if (l0 == 0) { u32x4 z = {0, 0, 0, 0}; st16(ws + WS_UR + (size_t)(d0 + dd) * URROW + 4096 + gi * 16, z); }
    }
  }
}

NOPK __global__ __launch_bounds__(1024, 4) void k_pre(const u16* __restrict__ u, const u16* __restrict__ logA, const u16* __restrict__ Aim,
                                                const u16* __restrict__ Cr, const u16* __restrict__ Ci, const u16* __restrict__ logdt,
                                                const u16* __restrict__ x, const u16* __restrict__ w, const u16* __restrict__ wo, char* __restrict__ ws) {
  extern __shared__ __attribute__((aligned(16))) char lds[];
  const int wave = __builtin_amdgcn_readfirstlane(TID >> 6), b = BID;
  if (TID < 32) ((unsigned*)(lds + K1_CTR))[TID] = 0u;
  SYNC();
  const int xcd = b & 7, idx = b >> 3;
  const int mb = (xcd & 3) * 4 + (idx & 3), nb = (xcd >> 2) * 8 + (idx >> 2);
  if (wave < 4) {
    gemm_loader(b, mb, nb, wave, x, w, u, wo, ws, lds);
  } else if (wave < 8) {
    {
      unsigned long long a0, a1;
      asm volatile("s_getpc_b64 s[70:71]\n\ts_add_u32 s70, s70, _Z5k_prePKtS0_S0_S0_S0_S0_S0_S0_S0_Pc@rel32@lo+4\n\ts_addc_u32 s71, s71, _Z5k_prePKtS0_S0_S0_S0_S0_S0_S0_S0_Pc@rel32@hi+12\n\ts_mov_b64 %0, s[70:71]" : "=s"(a0) :: "s70", "s71", "scc");
      asm volatile("s_getpc_b64 s[70:71]\n\ts_add_u32 s70, s70, _Z5k_outPKcPKtS2_Pt@rel32@lo+4\n\ts_addc_u32 s71, s71, _Z5k_outPKcPKtS2_Pt@rel32@hi+12\n\ts_mov_b64 %0, s[70:71]" : "=s"(a1) :: "s70", "s71", "scc");
      const unsigned long long len = a1 - a0 + 2048, off = (unsigned long long)(TID - 256) * 128;
      if (a1 > a0 && off < len) {
        unsigned pf;
        asm volatile("global_load_dword %0, %1, off\n\ts_waitcnt vmcnt(0)" : "=v"(pf) : "v"(a0 + off) : "memory");
      }
    }
    if (nb < 10) gemm_consumer<0>(mb, nb, wave - 4, ws, lds);
    else if (nb == 10) gemm_consumer<1>(mb, nb, wave - 4, ws, lds);
    else gemm_consumer<3>(mb, nb, wave - 4, ws, lds);
  } else {
    aux_waves(b, (wave - 8) >> 2, (wave - 8) & 3, u, logA, Aim, Cr, Ci, logdt, wo, ws, lds);
  }
}

constexpr int LDS_CP = 2 * CHIMG;
constexpr int LDS_AP = LDS_CP + 4 * 2304 * 4;
constexpr int LDS_FL = LDS_AP + 4 * 2 * 9 * 64 * 4;
constexpr int LDS_K2 = LDS_FL + 64;

#define GLOAD(dst, voff, sbase, off) asm volatile("global_load_dwordx4 %0, %1, %2 offset:" #off : "=v"(dst) : "v"(voff), "s"(sbase))
DI void exp_pack(const f32x16& s, bf16x8& pa, bf16x8& pb) {
  u32x4 a, b;
#pragma unroll
  for (int j = 0; j < 4; ++j) a[j] = pack2(__builtin_amdgcn_exp2f(s[2 * j]), __builtin_amdgcn_exp2f(s[2 * j + 1]));
#pragma unroll
  for (int j = 0; j < 4; ++j) b[j] = pack2(__builtin_amdgcn_exp2f(s[8 + 2 * j]), __builtin_amdgcn_exp2f(s[8 + 2 * j + 1]));
  pa = __builtin_bit_cast(bf16x8, a); pb = __builtin_bit_cast(bf16x8, b);
}
#define EP2(s_, j_) pack2(__builtin_amdgcn_exp2f((s_)[2 * (j_)]), __builtin_amdgcn_exp2f((s_)[2 * (j_) + 1]))
DI void attn_step(const char*& kp, const char*& vp, unsigned ko, unsigned vo, bf16x8& kc, bf16x8& kn, bf16x8& vca, bf16x8& vcb, bf16x8& vna, bf16x8& vnb,
                  bf16x8 q0, bf16x8 q1, const f32x16& nm0, const f32x16& nm1, f32x16& s0, f32x16& o0, f32x16& o1) {
  u32x4 pa, pb;
  pa[0] = EP2(s0, 0); pa[1] = EP2(s0, 1);
  SBAR();
  f32x16 s1 = MFMA32(kc, q1, nm1);
  SBAR();
  GLOAD(vna, vo, vp, 1152); GLOAD(vnb, vo, vp, 1728);
  GLOAD(kc, ko, kp, 2048);
  kp += 1024; vp += 2 * VSTEP;
  SBAR();
  pa[2] = EP2(s0, 2); pa[3] = EP2(s0, 3);
  asm volatile("s_waitcnt vmcnt(3)" : "+v"(vca), "+v"(vcb), "+v"(kn));
  SBAR();
  o0 = MFMA32(vca, __builtin_bit_cast(bf16x8, pa), o0);
  SBAR();
  pb[0] = EP2(s0, 4); pb[1] = EP2(s0, 5); pb[2] = EP2(s0, 6); pb[3] = EP2(s0, 7);
  SBAR();
  o0 = MFMA32(vcb, __builtin_bit_cast(bf16x8, pb), o0);
  SBAR();
  pa[0] = EP2(s1, 0); pa[1] = EP2(s1, 1);
  SBAR();
  s0 = MFMA32(kn, q0, nm0);
  SBAR();
  pa[2] = EP2(s1, 2); pa[3] = EP2(s1, 3);
  SBAR();
  o1 = MFMA32(vca, __builtin_bit_cast(bf16x8, pa), o1);
  SBAR();
  pb[0] = EP2(s1, 4); pb[1] = EP2(s1, 5); pb[2] = EP2(s1, 6); pb[3] = EP2(s1, 7);
  SBAR();
  o1 = MFMA32(vcb, __builtin_bit_cast(bf16x8, pb), o1);
  SBAR();
}
#define ATTN_FIRST_LOADS() do { GLOAD(kA, ko, kp, 0); GLOAD(vAa, vo, vp, 0); GLOAD(vAb, vo, vp, 576); GLOAD(kB, ko, kp, 1024); } while (0)
DI void attn_main(const char* kp, const char* vp, unsigned ko, unsigned vo, bf16x8 q0, bf16x8 q1, const f32x16& nm0, const f32x16& nm1, f32x16& o0, f32x16& o1,
                  bf16x8& kA, bf16x8& kB, bf16x8& vAa, bf16x8& vAb) {
#pragma unroll
  for (int i = 0; i < 16; ++i) { o0[i] = 0.f; o1[i] = 0.f; }
  bf16x8 vBa, vBb;
  asm volatile("" : "+v"(q0), "+v"(q1));
  asm volatile("s_waitcnt vmcnt(3)" : "+v"(kA));
  f32x16 s0 = MFMA32(kA, q0, nm0);
  for (int it = 0; it < 16; ++it) {
    attn_step(kp, vp, ko, vo, kA, kB, vAa, vAb, vBa, vBb, q0, q1, nm0, nm1, s0, o0, o1);
    attn_step(kp, vp, ko, vo, kB, kA, vBa, vBb, vAa, vAb, q0, q1, nm0, nm1, s0, o0, o1);
  }
  asm volatile("s_waitcnt vmcnt(0)" : "+v"(kA), "+v"(kB), "+v"(vAa), "+v"(vAb), "+v"(vBa), "+v"(vBb));
}

NOPK __global__ __launch_bounds__(768, 3) void k_attn_conv(char* __restrict__ ws) {
  extern __shared__ __attribute__((aligned(16))) char lds[];
  const int tid = TID, wave = __builtin_amdgcn_readfirstlane(tid >> 6), lane = tid & 63, r = lane & 31, hh = lane >> 5;
  const int b = BID;
  const int h = (b & 7) * 4 + ((b >> 3) & 3), qt = b >> 5;
  const int qg = wave & 3, kh = (wave >> 2) & 1;
  bf16x8 q0, q1; f32x16 nm0, nm1, o0, o1;
  const char* kp = ws + WS_K + (size_t)(h * 64 + kh * 32) * 1024;
  const char* vp = ws + WS_V + (size_t)h * VHEAD + (size_t)(kh * 64) * VSTEP;
  const unsigned ko = lane * 16, vo = hh * 288 + (r < 17 ? r : 17) * 16;
  const int qtile = qt * 8 + qg * 2;
  const int cw = wave - 8, cch = cw >> 1, chalf = cw & 1;
  float cinv0 = 1.f, cinv1 = 1.f;
  bf16x8 kA, kB, vAa, vAb;
  f32x2 kmv; float n0, n1;
  if (wave < 8) {
    q0 = *(const bf16x8*)(ws + WS_Q + ((size_t)(h * 64 + qtile) * 64 + lane) * 16);
    q1 = *(const bf16x8*)(ws + WS_Q + ((size_t)(h * 64 + qtile + 1) * 64 + lane) * 16);
    kmv = ((const f32x2*)(ws + WS_KM))[h * 64 + lane];
    SBAR();
    ATTN_FIRST_LOADS();
  }
  if (tid >= 512 && tid < 528) ((unsigned*)(lds + LDS_FL))[tid - 512] = 0u;
  asm volatile("s_waitcnt lgkmcnt(0)" ::: "memory");
  __builtin_amdgcn_s_barrier();
  asm volatile("" ::: "memory");
  if (wave < 8) {
    n0 = 0.f; n1 = 0.f;
#pragma unroll
    for (int j = 0; j < 4; ++j) {
      const unsigned a = __builtin_bit_cast(u32x4, q0)[j], c = __builtin_bit_cast(u32x4, q1)[j];
      n0 = __builtin_fmaf(bf_lo(a), bf_lo(a), __builtin_fmaf(bf_hi(a), bf_hi(a), n0));
      n1 = __builtin_fmaf(bf_lo(c), bf_lo(c), __builtin_fmaf(bf_hi(c), bf_hi(c), n1));
    }
    n0 = add_other_half(n0); n1 = add_other_half(n1);
    float km = row16_max(__builtin_fmaxf(kmv[0], kmv[1]));
    km = __builtin_fmaxf(__builtin_fmaxf(__builtin_bit_cast(float, __builtin_amdgcn_readlane(__builtin_bit_cast(int, km), 0)), __builtin_bit_cast(float, __builtin_amdgcn_readlane(__builtin_bit_cast(int, km), 16))),
               __builtin_fmaxf(__builtin_bit_cast(float, __builtin_amdgcn_readlane(__builtin_bit_cast(int, km), 32)), __builtin_bit_cast(float, __builtin_amdgcn_readlane(__builtin_bit_cast(int, km), 48))));
    const float m0 = __builtin_amdgcn_sqrtf(n0 * km) * 1.01f, m1 = __builtin_amdgcn_sqrtf(n1 * km) * 1.01f;
#pragma unroll
    for (int i = 0; i < 16; ++i) { nm0[i] = -m0; nm1[i] = -m1; }
  } else {
    __builtin_amdgcn_s_setprio(3);
    const int ct = chalf * 64 + lane;
    char* img = lds + cch * CHIMG;
    u16* tabl = (u16*)(lds + LDS_CP + cch * 2 * 2304 * 4);
    unsigned* mxs = (unsigned*)(lds + LDS_FL) + 12 + cch * 2;
    const char* ur = ws + WS_UR + (size_t)(2 * b + cch) * URROW;
    const u32x4* kt = (const u32x4*)(ws + WS_KT + (size_t)(2 * b + cch) * 3072);
    u32x4 tv0 = kt[ct], tv1 = {0, 0, 0, 0};
    if (ct < 36) tv1 = kt[128 + ct];
    u32x4 ua[3]; u32x2 ub[3]; u32x4 mp[2];
#pragma unroll
    for (int i = 0; i < 3; ++i) {
      const int g = ct + 128 * i;
      ua[i] = u32x4{0, 0, 0, 0}; ub[i] = u32x2{0, 0};
      if (g < 260) { ua[i] = *(const u32x4*)(ur + g * 16); ub[i] = *(const u32x2*)(ur + g * 16 + 16); }
    }
#pragma unroll
    for (int i = 0; i < 2; ++i) mp[i] = *(const u32x4*)(KMAP.v + (ct + 128 * i) * 8);
    { u32x4 z = {0, 0, 0, 0};
      *(u32x4*)(img + UIMG + ct * 16) = z; if (ct < 32) *(u32x4*)(img + UIMG + 2048 + ct * 16) = z; }
    unsigned mu = 0, mk = 0;
#pragma unroll
    for (int i = 0; i < 3; ++i)
#pragma unroll
      for (int j = 0; j < 4; ++j) { const unsigned a = ua[i][j] & 0x7fff7fffu; mu = UMAX(mu, UMAX(a << 16, a & 0xffff0000u)); }
#pragma unroll
    for (int j = 0; j < 4; ++j) mk = UMAX(mk, UMAX(tv0[j] & 0x7fffffffu, tv1[j] & 0x7fffffffu));
    mu = F2U(row16_max(U2F(mu & 0x7f800000u))); mk = F2U(row16_max(U2F(mk & 0x7f800000u)));
    if ((lane & 15) == 0) {
      asm volatile("ds_max_u32 %0, %1\n\tds_max_u32 %0, %2 offset:4" :: "v"((unsigned)(size_t)mxs), "v"(mu), "v"(mk) : "memory");
    }
    lds_barrier((unsigned*)(lds + LDS_FL) + 8 + cch, 2);
    const int eu = (int)(mxs[0] >> 23), ek = (int)(mxs[1] >> 23);
    const int su_e = 268 - eu > 253 ? 253 : 268 - eu, sk_e = 268 - ek > 253 ? 253 : 268 - ek;
    const float su = U2F((unsigned)su_e << 23), sk = U2F((unsigned)sk_e << 23);
    cinv0 = U2F((unsigned)(254 - su_e) << 23); cinv1 = U2F((unsigned)(254 - sk_e) << 23);
#define CVH(x_) __builtin_bit_cast(unsigned, __builtin_amdgcn_cvt_pkrtz(bf_lo(x_) * su, bf_hi(x_) * su))
#pragma unroll
    for (int i = 0; i < 3; ++i) {
      const int g = ct + 128 * i;
      if (g < 260) {
        const unsigned h0 = CVH(ua[i][0]), h1 = CVH(ua[i][1]), h2 = CVH(ua[i][2]), h3 = CVH(ua[i][3]), h4 = CVH(ub[i][0]), h5 = CVH(ub[i][1]);
        const unsigned a0 = __builtin_amdgcn_alignbit(h1, h0, 16), a1 = __builtin_amdgcn_alignbit(h2, h1, 16), a2 = __builtin_amdgcn_alignbit(h3, h2, 16),
                       a3 = __builtin_amdgcn_alignbit(h4, h3, 16), a4 = __builtin_amdgcn_alignbit(h5, h4, 16);
        const u32x4 c0 = {h0, h1, h2, h3}, c1 = {a0, a1, a2, a3}, c2 = {h1, h2, h3, h4}, c3 = {a1, a2, a3, a4};
        *(u32x4*)(img + g * 16) = c0; *(u32x4*)(img + UCOPY + g * 16) = c1;
        *(u32x4*)(img + 2 * UCOPY + g * 16) = c2; *(u32x4*)(img + 3 * UCOPY + g * 16) = c3;
      }
    }
    {
      typedef _Float16 h2_t __attribute__((ext_vector_type(2)));
      u32x2 t0, t1;
#pragma unroll
      for (int j = 0; j < 2; ++j) {
        h2_t a = {(_Float16)(U2F(tv0[2 * j]) * sk), (_Float16)(U2F(tv0[2 * j + 1]) * sk)}, c = {(_Float16)(U2F(tv1[2 * j]) * sk), (_Float16)(U2F(tv1[2 * j + 1]) * sk)};
        t0[j] = __builtin_bit_cast(unsigned, a); t1[j] = __builtin_bit_cast(unsigned, c);
      }
      *(u32x2*)(tabl + ct * 4) = t0;
      if (ct < 64) *(u32x2*)(tabl + 512 + ct * 4) = t1;
    }
    lds_barrier((unsigned*)(lds + LDS_FL) + 8 + cch, 4);
#pragma unroll
    for (int i = 0; i < 2; ++i) {
      const int g = ct + 128 * i;
      u32x4 vh;
#pragma unroll
      for (int j = 0; j < 4; ++j) vh[j] = (unsigned)tabl[mp[i][j] & 0xffff] | ((unsigned)tabl[mp[i][j] >> 16] << 16);
      *(u32x4*)(img + UIMG + (32 + (g >> 2)) * KROW + (g & 3) * 16) = vh;
    }
    lds_barrier((unsigned*)(lds + LDS_FL) + 8 + cch, 6);
    __builtin_amdgcn_s_setprio(0);
  }
  float* ap = (float*)(lds + LDS_AP);
  volatile int* fl = (volatile int*)(lds + LDS_FL);
  for (int pass = 0;; ++pass) {
    if (wave < 8) {
      attn_main(kp, vp, ko, vo, q0, q1, nm0, nm1, o0, o1, kA, kB, vAa, vAb);
      o0[8] = add_other_half(o0[8]); o1[8] = add_other_half(o1[8]);
      if (kh == 1) {
#pragma unroll
        for (int i = 0; i < 9; ++i) { ap[((qg * 2 + 0) * 9 + i) * 64 + lane] = o0[i]; ap[((qg * 2 + 1) * 9 + i) * 64 + lane] = o1[i]; }
      }
      const bool bad = !(o0[8] >= 8.2718061e-25f) || !(o1[8] >= 8.2718061e-25f);
      const int anybad = (__builtin_amdgcn_ballot_w64(bad) != 0);
      if (lane == 0) fl[wave] = anybad;
    } else if (pass == 0) {
      const int c = (3 - r) & 3;
      const char* ua = lds + cch * CHIMG + c * UCOPY + 2 * (2047 + 8 * hh - r - c);
      const char* kb = lds + cch * CHIMG + UIMG + (r + 32) * KROW + 16 * hh;
      f32x16 a0, a1;
#pragma unroll
      for (int i = 0; i < 16; ++i) { a0[i] = 0.f; a1[i] = 0.f; }
#pragma unroll 2
      for (int dd = 0; dd < 16; ++dd) {
        const int dl = chalf * 16 + dd;
#pragma unroll
        for (int s = 0; s < 2; ++s) {
          const char* up = ua - 64 * dl + 32 * s;
          const s16x4 x0 = *(const s16x4*)up, x1 = *(const s16x4*)(up + 8);
          const s16x4 y0 = *(const s16x4*)(up - 2048), y1 = *(const s16x4*)(up - 2048 + 8);
          const bf16x8 A0 = __builtin_shufflevector(x0, x1, 0, 1, 2, 3, 4, 5, 6, 7), A1 = __builtin_shufflevector(y0, y1, 0, 1, 2, 3, 4, 5, 6, 7);
          const char* kq = kb - KROW * dl + 32 * s;
          const bf16x8 B0 = *(const bf16x8*)kq, B1 = *(const bf16x8*)(kq + 32 * KROW);
          a0 = MFMA32H(A0, B0, a0); a1 = MFMA32H(A0, B1, a1);
          a1 = MFMA32H(A1, B0, a1);
        }
      }
      float* cp = (float*)(lds + LDS_CP) + cw * 2304;
#pragma unroll
      for (int g = 0; g < 4; ++g) {
        f32x4 v0 = {a0[4 * g], a0[4 * g + 1], a0[4 * g + 2], a0[4 * g + 3]}, v1 = {a1[4 * g], a1[4 * g + 1], a1[4 * g + 2], a1[4 * g + 3]};
        *(f32x4*)(cp + 36 * r + 8 * g + 4 * hh) = v0;
        *(f32x4*)(cp + 36 * (32 + r) + 8 * g + 4 * hh) = v1;
      }
      lds_barrier((unsigned*)(lds + LDS_FL) + 8 + cch, 8);
#pragma unroll
      for (int i = 0; i < 2; ++i) {
        const int g = chalf * 128 + i * 64 + lane;
        const float* c0 = (const float*)(lds + LDS_CP) + (2 * cch) * 2304 + 36 * (g >> 2) + 8 * (g & 3);
        const f32x4 a = (*(const f32x4*)c0 + *(const f32x4*)(c0 + 2304)) * cinv0 * cinv1, bq = (*(const f32x4*)(c0 + 4) + *(const f32x4*)(c0 + 2304 + 4)) * cinv0 * cinv1;
        u32x4 w = {pack2(a[0], a[1]), pack2(a[2], a[3]), pack2(bq[0], bq[1]), pack2(bq[2], bq[3])};
        st16(ws + WS_YT + ((size_t)(2 * b + cch) * L + 8 * g) * 2, w);
      }
    }
    SYNC();
    int redo = 0;
    if (pass == 0) {
#pragma unroll
      for (int i = 0; i < 8; ++i) redo |= fl[i];
    }
    if (!redo) break;
    if (wave < 8) {
      const char* kall = ws + WS_K + ((size_t)(h * 64) * 64 + lane) * 16;
      float m0 = -3.0e38f, m1 = -3.0e38f;
      f32x16 z;
#pragma unroll
      for (int i = 0; i < 16; ++i) z[i] = 0.f;
      for (int it = 0; it < 64; ++it) {
        const bf16x8 kf = *(const bf16x8*)(kall + it * 1024);
        const f32x16 s0 = MFMA32(kf, q0, z), s1 = MFMA32(kf, q1, z);
#pragma unroll
        for (int i = 0; i < 16; ++i) { m0 = __builtin_fmaxf(m0, s0[i]); m1 = __builtin_fmaxf(m1, s1[i]); }
      }
      m0 = max_other_half(m0); m1 = max_other_half(m1);
#pragma unroll
      for (int i = 0; i < 16; ++i) { nm0[i] = -m0; nm1[i] = -m1; }
      ATTN_FIRST_LOADS();
    }
    SYNC();
  }
  if (wave < 4) {
#pragma unroll
    for (int t = 0; t < 2; ++t) {
      float v[9];
#pragma unroll
      for (int i = 0; i < 9; ++i) v[i] = (t ? o1[i] : o0[i]) + ap[((qg * 2 + t) * 9 + i) * 64 + lane];
      const float inv = 1.f / v[8];
      u32x4 w;
#pragma unroll
      for (int j = 0; j < 4; ++j) w[j] = pack2(v[2 * j] * inv, v[2 * j + 1] * inv);
      st16(ws + WS_O + ((size_t)((qtile + t) * 32 + h) * 64 + lane) * 16, w);
    }
  }
}

constexpr int RED_STRIDE = 68;
DI void out_role(int b, const char* ws, u16* out, char* lds) {
  const int tid = TID, wave = tid >> 6, lane = tid & 63, r = lane & 31, hh = lane >> 5;
  const int xcd = b & 7, idx = b >> 3, mt = xcd * 4 + (idx & 3), nt = idx >> 2;
  bf16x8 of[2][8], wf[2][8];
#pragma unroll
  for (int k = 0; k < 8; ++k) {
    const int h = wave * 8 + k;
#pragma unroll
    for (int i = 0; i < 2; ++i) {
      of[i][k] = *(const bf16x8*)(ws + WS_O + ((size_t)((2 * mt + i) * 32 + h) * 64 + lane) * 16);
      wf[i][k] = *(const bf16x8*)(ws + WS_WO + ((size_t)((2 * nt + i) * 32 + h) * 64 + lane) * 16);
    }
  }
  f32x16 acc[2][2];
#pragma unroll
  for (int i = 0; i < 2; ++i)
#pragma unroll
    for (int j = 0; j < 2; ++j)
#pragma unroll
      for (int e = 0; e < 16; ++e) acc[i][j][e] = 0.f;
#pragma unroll
  for (int k = 0; k < 8; ++k)
#pragma unroll
    for (int i = 0; i < 2; ++i)
#pragma unroll
      for (int j = 0; j < 2; ++j) acc[i][j] = MFMA32(of[i][k], wf[j][k], acc[i][j]);
  float* red = (float*)lds + wave * 64 * RED_STRIDE;
#pragma unroll
  for (int i = 0; i < 2; ++i)
#pragma unroll
    for (int j = 0; j < 2; ++j)
#pragma unroll
      for (int e = 0; e < 16; ++e) red[(32 * i + crow(e, hh)) * RED_STRIDE + 32 * j + r] = acc[i][j][e];
  SYNC();
  const int tok = tid >> 2, n0 = (tid & 3) * 16;
  const float* rp = (const float*)lds + tok * RED_STRIDE + n0;
  u32x4 w[2];
#pragma unroll
  for (int g = 0; g < 4; ++g) {
    f32x4 s = *(const f32x4*)(rp + 4 * g);
#pragma unroll
    for (int wv = 1; wv < 4; ++wv) s += *(const f32x4*)(rp + wv * 64 * RED_STRIDE + 4 * g);
    w[g >> 1][(g & 1) * 2] = pack2(s[0], s[1]); w[g >> 1][(g & 1) * 2 + 1] = pack2(s[2], s[3]);
  }
  u16* op = out + (size_t)(mt * 64 + tok) * 1024 + 512 + nt * 64 + n0;
  *(u32x4*)op = w[0]; *(u32x4*)(op + 8) = w[1];
}
DI void fin_role(int b, const char* ws, const u16* u, const u16* dsk, u16* out, char* lds) {
  const int tid = TID;
  const int l0 = (b >> 3) * 64, d0 = (b & 7) * 64;
  u16* t = (u16*)lds;
#pragma unroll
  for (int i = 0; i < 2; ++i) {
    const int idx = tid + 256 * i, row = idx >> 3, ch = idx & 7;
    *(u32x4*)(t + row * 72 + ch * 8) = *(const u32x4*)(ws + WS_YT + ((size_t)(d0 + row) * L + l0 + ch * 8) * 2);
  }
  SYNC();
#pragma unroll
  for (int i = 0; i < 2; ++i) {
    const int idx = tid + 256 * i, ll = idx >> 3, ch = idx & 7;
    const u32x4 uv = *(const u32x4*)(u + (size_t)(l0 + ll) * DM + d0 + ch * 8);
    const u32x4 dv = *(const u32x4*)(dsk + d0 + ch * 8);
    u32x4 v;
#pragma unroll
    for (int j = 0; j < 4; ++j) {
      const float y0 = bf2f(t[(ch * 8 + 2 * j) * 72 + ll]), y1 = bf2f(t[(ch * 8 + 2 * j + 1) * 72 + ll]);
      const unsigned sk = pack2(bf_lo(uv[j]) * bf_lo(dv[j]), bf_hi(uv[j]) * bf_hi(dv[j]));
      v[j] = pack2(y0 + bf_lo(sk), y1 + bf_hi(sk));
    }
    *(u32x4*)(out + (size_t)(l0 + ll) * 1024 + d0 + ch * 8) = v;
  }
}
NOPK __global__ __launch_bounds__(256) void k_out(const char* ws, const u16* u, const u16* dsk, u16* out) {
  extern __shared__ __attribute__((aligned(16))) char lds[];
  if (BID < 256) out_role(BID, ws, out, lds);
  else fin_role(BID - 256, ws, u, dsk, out, lds);
}

extern "C" void kernel_launch(void* const* d_in, const int* in_sizes, int n_in, void* d_out, int out_size, void* d_ws, size_t ws_size,
                              hipStream_t stream) {
  const u16* u = (const u16*)d_in[0]; const u16* logA = (const u16*)d_in[1]; const u16* Aim = (const u16*)d_in[2];
  const u16* Cr = (const u16*)d_in[3]; const u16* Ci = (const u16*)d_in[4]; const u16* logdt = (const u16*)d_in[5];
  const u16* dsk = (const u16*)d_in[6]; const u16* x = (const u16*)d_in[7]; const u16* wqkv = (const u16*)d_in[8]; const u16* wo = (const u16*)d_in[9];
  u16* out = (u16*)d_out; char* ws = (char*)d_ws;
  constexpr int LDS_OUT = 4 * 64 * RED_STRIDE * 4;
  (void)hipFuncSetAttribute((const void*)k_pre, hipFuncAttributeMaxDynamicSharedMemorySize, LDS_K1);
  (void)hipFuncSetAttribute((const void*)k_attn_conv, hipFuncAttributeMaxDynamicSharedMemorySize, LDS_K2);
  (void)hipFuncSetAttribute((const void*)k_out, hipFuncAttributeMaxDynamicSharedMemorySize, LDS_OUT);
  k_pre<<<256, 1024, LDS_K1, stream>>>(u, logA, Aim, Cr, Ci, logdt, x, wqkv, wo, ws);
  k_attn_conv<<<256, 768, LDS_K2, stream>>>(ws);
  k_out<<<512, 256, LDS_OUT, stream>>>(ws, u, dsk, out);
}
```
